```python
import torch

def _softmax(x, axis=-1):
    return torch.softmax(x, dim=axis)
def _gelu(x):
    return torch.nn.functional.gelu(x, approximate="tanh")
def _silu(x):
    return torch.nn.functional.silu(x)
def _sigmoid(x):
    return torch.sigmoid(x)
def _layernorm(x, axis=-1, eps=1e-5):
    m = x.mean(dim=axis, keepdim=True)
    v = x.var(dim=axis, keepdim=True, unbiased=False)
    return (x - m) / torch.sqrt(v + eps)
def _scatter_add(tgt, idx, src, axis=0):
    return tgt.clone().index_add_(axis, idx, src)
def _segment_sum(src, seg, n):
    out = torch.zeros((n,) + src.shape[1:], dtype=src.dtype)
    return out.index_add_(0, seg, src)
def _pad(x, pw):
    flat = tuple(v for pair in reversed(pw) for v in pair)
    return torch.nn.functional.pad(x, flat)

kernel_name = 'parallel_sum__mha_block__dense_activation_gelu__prompt'

def setup_inputs():

    _w = (lambda a: (lambda t: t.to(torch.bfloat16) if t.is_floating_point() else t)(torch.as_tensor(a)))
    def _A_setup():
        B, S, D, H = 1, 2048, 640, 32
        return {
            "x": _w((lambda *s: torch.randn(*s, dtype=torch.bfloat16) * 0.05)(B, S, D)),
            "wqkv": _w((lambda *s: torch.randn(*s, dtype=torch.bfloat16) * 0.05)(D, 3 * D)),
            "wo": _w((lambda *s: torch.randn(*s, dtype=torch.bfloat16) * 0.05)(D, D)),
        }
    _A_in = _A_setup()

    def _B_setup():
        B, S, D, F = 1, 2048, 640, 1728
        return {
            "x": _w((lambda *s: torch.randn(*s, dtype=torch.bfloat16) * 0.05)(B, S, D)),
            "w1": _w((lambda *s: torch.randn(*s, dtype=torch.bfloat16) * 1.6)(D, F) * (D ** -0.5)),
            "b1": _w((lambda *s: torch.randn(*s, dtype=torch.bfloat16) * 1.6)(F) * 0.01),
            "w2": _w((lambda *s: torch.randn(*s, dtype=torch.bfloat16) * 0.2)(F, D) * (F ** -0.5)),
            "b2": _w((lambda *s: torch.randn(*s, dtype=torch.bfloat16) * 0.2)(D) * 0.01),
        }
    _B_in = _B_setup()

    return {
        "A_x": _A_in["x"],
        "A_wqkv": _A_in["wqkv"],
        "A_wo": _A_in["wo"],
        "B_x": _B_in["x"],
        "B_w1": _B_in["w1"],
        "B_b1": _B_in["b1"],
        "B_w2": _B_in["w2"],
        "B_b2": _B_in["b2"],
    }

def reference(A_x, A_wqkv, A_wo, B_x, B_w1, B_b1, B_w2, B_b2):
    def _A_ref(x, wqkv, wo):
        B, S, D = x.shape
        H = 32
        Dh = D // H
        qkv = (lambda x, *d: x.permute(*d))((x @ wqkv).reshape(B, S, 3, H, Dh), 2, 0, 3, 1, 4)
        q, k, v = qkv[0], qkv[1], qkv[2]
        scores = torch.einsum("bhqd,bhkd->bhqk", q, k) * (Dh ** -0.5)
        p = _softmax(scores, axis=-1)
        o = torch.einsum("bhqk,bhkd->bhqd", p, v)
        return (lambda x, *d: x.permute(*d))(o, 0, 2, 1, 3).reshape(B, S, D) @ wo

    def _B_ref(x, w1, b1, w2, b2):
        h = torch.einsum("bsd,df->bsf", x, w1) + b1
        h = _gelu(h)
        return torch.einsum("bsf,fd->bsd", h, w2) + b2

    x = A_x
    xn = _layernorm(x, axis=-1)
    return x + _A_ref(xn, A_wqkv, A_wo) + _B_ref(xn, B_w1, B_b1, B_w2, B_b2)
```

```cpp
#include <hip/hip_runtime.h>
#include <hip/hip_bf16.h>
#include <stdint.h>

typedef __bf16 bf2 __attribute__((ext_vector_type(2)));
typedef __bf16 bf8 __attribute__((ext_vector_type(8)));
typedef short s4 __attribute__((ext_vector_type(4)));
typedef float f2 __attribute__((ext_vector_type(2)));
typedef float f4 __attribute__((ext_vector_type(4)));
typedef float f16v __attribute__((ext_vector_type(16)));
typedef unsigned u2 __attribute__((ext_vector_type(2)));
typedef unsigned u4 __attribute__((ext_vector_type(4)));
typedef unsigned short u16;
#define LDS_AS __attribute__((address_space(3)))
#define GLB_AS __attribute__((address_space(1)))
#define DEVINL __device__ __forceinline__

constexpr int SEQ = 2048, DM = 640, NH = 32, FF = 1728;
constexpr size_t OFF_Q  = 0;
constexpr size_t OFF_KV = OFF_Q  + (size_t)32 * 64 * 1280;
constexpr size_t OFF_H  = OFF_KV + (size_t)32 * 64 * 3072;
constexpr size_t OFF_W2 = OFF_H  + (size_t)64 * 216 * 512;
constexpr size_t OFF_WO = OFF_W2 + (size_t)20 * 216 * 512;
constexpr size_t OFF_O  = OFF_WO + (size_t)20 * 80 * 512;
constexpr size_t OFF_PB = OFF_O  + (size_t)64 * 80 * 512;

#define WAIT_V(n) asm volatile("s_waitcnt vmcnt(" #n ")" ::: "memory")
#define WAIT_L0() asm volatile("s_waitcnt lgkmcnt(0)" ::: "memory")
#define BARRIER() do { asm volatile("" ::: "memory"); __builtin_amdgcn_s_barrier(); asm volatile("" ::: "memory"); } while (0)

DEVINL unsigned pk_bf16(float a, float b) {
  f2 f = {a, b};
  bf2 r = __builtin_convertvector(f, bf2);
  return __builtin_bit_cast(unsigned, r);
}
DEVINL float bf_lo(unsigned v) { return __builtin_bit_cast(float, v << 16); }
DEVINL float bf_hi(unsigned v) { return __builtin_bit_cast(float, v & 0xffff0000u); }
DEVINL f16v mfma16(u4 a, u4 b, f16v c) {
  return __builtin_amdgcn_mfma_f32_32x32x16_bf16(__builtin_bit_cast(bf8, a), __builtin_bit_cast(bf8, b), c, 0, 0, 0);
}
DEVINL f16v mfma8(u2 a, u2 b, f16v c) {
  return __builtin_amdgcn_mfma_f32_32x32x8bf16_1k(__builtin_bit_cast(s4, a), __builtin_bit_cast(s4, b), c, 0, 0, 0);
}
DEVINL float dot2(unsigned a, unsigned b, float c) {
  return __builtin_amdgcn_fdot2_f32_bf16(__builtin_bit_cast(bf2, a), __builtin_bit_cast(bf2, b), c, false);
}
DEVINL float xor32(float v) { return __shfl_xor(v, 32); }
DEVINL void glds16(const char* g, char* l) {
  __builtin_amdgcn_global_load_lds((const GLB_AS void*)g, (LDS_AS void*)l, 16, 0, 0);
}
DEVINL u2 tr_read(const char* l) {
  s4 r = __builtin_amdgcn_ds_read_tr16_b64_v4i16((LDS_AS s4*)l);
  return __builtin_bit_cast(u2, r);
}

constexpr int K1_STAGE = 49152;
constexpr int K1_STATS = 3 * K1_STAGE;
constexpr int K1_LDS = K1_STATS + 2048;

struct K1Ctx {
  const char* xg;
  const char* wg;
  int ldw2;
  unsigned aoff[2], boff[4];
};

DEVINL void k1_issue(const K1Ctx& c, char* smem, int k, int buf, int w) {
  char* st = smem + buf * K1_STAGE;
  const char* xa = c.xg + k * 128;
  const char* wb = c.wg + (size_t)k * 64 * c.ldw2;
#pragma unroll
  for (int i = 0; i < 2; ++i) glds16(xa + c.aoff[i], st + (2 * w + i) * 1024);
#pragma unroll
  for (int i = 0; i < 4; ++i) glds16(wb + c.boff[i], st + 16384 + (4 * w + i) * 1024);
}

template <int NTW, int KIND, int WN>
DEVINL void k1_body(const K1Ctx& c, char* smem, int w, int lane, int rb, int col0, int t0,
                    const u16* b1, char* ws) {
  constexpr bool SWAP = (KIND != 1);
  const int wm = w & 3, r = lane & 31, hi = lane >> 5;
  f16v acc[NTW];
#pragma unroll
  for (int j = 0; j < NTW; ++j) acc[j] = f16v{};
  float rs = 0.f, cs = 0.f;
  int aaddr[4], baddr[NTW], tile[NTW];
  {
    const int row = wm * 32 + r;
#pragma unroll
    for (int s = 0; s < 4; ++s) aaddr[s] = row * 128 + 16 * ((2 * s + hi) ^ ((row >> 1) & 7));
    const int blk = (lane >> 4) & 1, q = (lane & 15) >> 2, p = lane & 3;
#pragma unroll
    for (int j = 0; j < NTW; ++j) {
      int jj = j + wm; jj = jj >= NTW ? jj - NTW : jj; jj = jj >= NTW ? jj - NTW : jj;
      tile[j] = t0 + jj;
      baddr[j] = 16384 + (8 * hi + q) * 512 + ((tile[j] ^ q) * 64) + blk * 32 + p * 8;
    }
  }
  int buf = 0;
  for (int k = 0; k < 10; ++k) {
    if (k == 0) WAIT_V(12); else if (k == 9) WAIT_V(0); else WAIT_V(6);
    BARRIER();
    if (k >= 1 && k <= 7) k1_issue(c, smem, k + 2, buf == 0 ? 2 : buf - 1, w);
    const char* st = smem + buf * K1_STAGE;
#pragma unroll
    for (int s = 0; s < 4; ++s) {
      u4 a = *(const u4*)(st + aaddr[s]);
      u4 b[NTW];
#pragma unroll
      for (int j = 0; j < NTW; ++j) {
        u2 lo = tr_read(st + baddr[j] + s * 8192);
        u2 hh = tr_read(st + baddr[j] + s * 8192 + 2048);
        b[j] = u4{lo[0], lo[1], hh[0], hh[1]};
      }
#pragma unroll
      for (int j = 0; j < NTW; ++j) acc[j] = SWAP ? mfma16(b[j], a, acc[j]) : mfma16(a, b[j], acc[j]);
#pragma unroll
      for (int e = 0; e < 4; ++e) {
        rs = dot2(a[e], WN ? a[e] : 0x3f803f80u, rs);
        cs = dot2(b[0][e], 0x3f803f80u, cs);
      }
    }
    WAIT_L0();
    buf = buf == 2 ? 0 : buf + 1;
  }
  float* stats = (float*)(smem + K1_STATS);
  rs += xor32(rs);
  cs += xor32(cs);
  if (hi == 0) {
    stats[WN * 128 + wm * 32 + r] = rs;
    if (wm < NTW) stats[256 + tile[0] * 32 + r] = cs;
  }
  WAIT_L0();
  BARRIER();
  const int rt = rb * 4 + wm;
  if constexpr (SWAP) {
    const float mu = stats[wm * 32 + r] * (1.f / DM);
    const float var = fmaxf(stats[128 + wm * 32 + r] * (1.f / DM) - mu * mu, 0.f);
    const float rstd = __builtin_amdgcn_rsqf(var + 1e-5f);
#pragma unroll
    for (int j = 0; j < NTW; ++j) {
#pragma unroll
      for (int g = 0; g < 4; ++g) {
        const int nl = tile[j] * 32 + 8 * g + 4 * hi;
        const int n = col0 + nl;
        const f4 csv = *(const f4*)(stats + 256 + nl);
        float v[4];
#pragma unroll
        for (int e = 0; e < 4; ++e) v[e] = (acc[j][4 * g + e] - mu * csv[e]) * rstd;
        if constexpr (KIND == 0) {
          const bool isq = n < DM;
          const int nn = isq ? n : n - DM;
          const int head = nn / 20, d = nn - head * 20;
          const float sc = isq ? 0.22360679774997896f * 1.4426950408889634f : 1.f;
          u2 o = {pk_bf16(v[0] * sc, v[1] * sc), pk_bf16(v[2] * sc, v[3] * sc)};
          char* base = isq ? ws + OFF_Q + (size_t)(head * 64 + rt) * 1280 : ws + OFF_KV + (size_t)(head * 64 + rt) * 3072;
          const int off = d < 16 ? r * 16 + (d >> 3) * 512 + (d & 7) * 2 : 1024 + r * 8;
          *(u2*)(base + off) = o;
          if (!isq && d == 16) *(u2*)(base + 1280 + r * 8) = u2{0x3f803f80u, 0x00003f80u};
        } else {
          const u2 bb = *(const u2*)(b1 + n);
          v[0] += bf_lo(bb[0]); v[1] += bf_hi(bb[0]); v[2] += bf_lo(bb[1]); v[3] += bf_hi(bb[1]);
#pragma unroll
          for (int e = 0; e < 4; ++e) {
            const float t = v[e] * (-2.3022081986f - 0.1029432397f * v[e] * v[e]);
            v[e] = v[e] * __builtin_amdgcn_rcpf(1.f + __builtin_amdgcn_exp2f(t));
          }
          u2 o = {pk_bf16(v[0], v[1]), pk_bf16(v[2], v[3])};
          *(u2*)(ws + OFF_H + ((size_t)(rt * 216 + (n >> 3)) * 32 + r) * 16 + hi * 8) = o;
        }
      }
    }
  } else {
    float mu[16], rstd[16];
#pragma unroll
    for (int g = 0; g < 4; ++g) {
      const f4 sm = *(const f4*)(stats + wm * 32 + 8 * g + 4 * hi);
      const f4 sq = *(const f4*)(stats + 128 + wm * 32 + 8 * g + 4 * hi);
#pragma unroll
      for (int e = 0; e < 4; ++e) {
        const float m = sm[e] * (1.f / DM);
        mu[4 * g + e] = m;
        rstd[4 * g + e] = __builtin_amdgcn_rsqf(fmaxf(sq[e] * (1.f / DM) - m * m, 0.f) + 1e-5f);
      }
    }
#pragma unroll
    for (int j = 0; j < NTW; ++j) {
      const int nl = tile[j] * 32 + r;
      const int nn = col0 + nl - 2 * DM;
      const int head = nn / 20, d = nn - head * 20;
      const float csv = stats[256 + nl];
      char* base = ws + OFF_KV + (size_t)(head * 64 + rt) * 3072 + 1536 + hi * 384;
#pragma unroll
      for (int s = 0; s < 2; ++s) {
        u4 o;
#pragma unroll
        for (int e = 0; e < 4; ++e) {
          const int i0 = 8 * s + 2 * e, i1 = i0 + 1;
          o[e] = pk_bf16((acc[j][i0] - mu[i0] * csv) * rstd[i0], (acc[j][i1] - mu[i1] * csv) * rstd[i1]);
        }
        *(u4*)(base + s * 768 + d * 16) = o;
        if (d < 4) {
          const unsigned cst = d == 0 ? 0x3f803f80u : 0u;
          *(u4*)(base + s * 768 + (20 + d) * 16) = u4{cst, cst, cst, cst};
        }
      }
    }
  }
}

__global__ __launch_bounds__(512, 2) void k1_kernel(const u16* __restrict__ x, const u16* __restrict__ wqkv,
                                                     const u16* __restrict__ w1, const u16* __restrict__ b1,
                                                     const u16* __restrict__ w2, const u16* __restrict__ wo,
                                                     char* __restrict__ ws) {
  __shared__ __attribute__((aligned(16))) char smem[K1_LDS];
  const int tid = threadIdx.x, lane = tid & 63;
  const int w = __builtin_amdgcn_readfirstlane(tid >> 6);
  const int b = blockIdx.x, xcd = b & 7, idx = b >> 3;
  const int rb = (xcd & 1) * 8 + (idx & 7);
  const int cc = idx >> 3;
  const int chunk = (xcd >> 1) * 2 + (cc & 1);
  const bool isw1 = cc >= 2;
  int col0, ntiles;
  if (!isw1) { col0 = chunk * 256; ntiles = chunk < 7 ? 8 : 4; }
  else { col0 = (chunk < 6 ? 7 * chunk : 42 + 6 * (chunk - 6)) * 32; ntiles = chunk < 6 ? 7 : 6; }

  unsigned pw[8];
  const int item = b * 512 + tid;
  const int kc = item / 320, np = item - kc * 320;
  const bool do_tile = b < 185;
  if (do_tile) {
    const u16* src = (kc < 216 ? w2 + (size_t)kc * 8 * DM : wo + (size_t)(kc - 216) * 8 * DM) + 2 * np;
#pragma unroll
    for (int j = 0; j < 8; ++j) pw[j] = *(const unsigned*)(src + j * DM);
  }
  asm volatile("" ::: "memory");

  K1Ctx c;
  c.ldw2 = isw1 ? FF * 2 : 3 * DM * 2;
  c.xg = (const char*)(x + (size_t)rb * 128 * DM);
  c.wg = (const char*)((isw1 ? w1 : wqkv) + col0);
#pragma unroll
  for (int i = 0; i < 2; ++i) {
    const int row = 16 * w + 8 * i + (lane >> 3);
    c.aoff[i] = row * (DM * 2) + 16 * ((lane & 7) ^ ((row >> 1) & 7));
  }
#pragma unroll
  for (int i = 0; i < 4; ++i) {
    const int row = 2 * (4 * w + i) + (lane >> 5);
    int ch = (lane & 31) ^ ((row & 3) << 2);
    ch = ch >= ntiles * 4 ? ch - 16 : ch;
    c.boff[i] = row * c.ldw2 + ch * 16;
  }
  k1_issue(c, smem, 0, 0, w);
  k1_issue(c, smem, 1, 1, w);
  k1_issue(c, smem, 2, 2, w);

  const int wn = w >> 2;
  if (!isw1) {
    if (chunk < 5) {
      if (wn == 0) k1_body<4, 0, 0>(c, smem, w, lane, rb, col0, 0, b1, ws);
      else         k1_body<4, 0, 1>(c, smem, w, lane, rb, col0, 4, b1, ws);
    } else if (chunk < 7) {
      if (wn == 0) k1_body<4, 1, 0>(c, smem, w, lane, rb, col0, 0, b1, ws);
      else         k1_body<4, 1, 1>(c, smem, w, lane, rb, col0, 4, b1, ws);
    } else {
      if (wn == 0) k1_body<2, 1, 0>(c, smem, w, lane, rb, col0, 0, b1, ws);
      else         k1_body<2, 1, 1>(c, smem, w, lane, rb, col0, 2, b1, ws);
    }
  } else {
    if (wn == 0) {
      if (ntiles == 7) k1_body<4, 2, 0>(c, smem, w, lane, rb, col0, 0, b1, ws);
      else             k1_body<3, 2, 0>(c, smem, w, lane, rb, col0, 0, b1, ws);
    } else {
      k1_body<3, 2, 1>(c, smem, w, lane, rb, col0, ntiles - 3, b1, ws);
    }
  }

  if (do_tile) {
    u4 lo, hh;
#pragma unroll
    for (int e = 0; e < 4; ++e) {
      lo[e] = __builtin_amdgcn_perm(pw[2 * e + 1], pw[2 * e], 0x05040100u);
      hh[e] = __builtin_amdgcn_perm(pw[2 * e + 1], pw[2 * e], 0x07060302u);
    }
    const int n = 2 * np, ct = n >> 5, cl = n & 31;
    char* dst = kc < 216 ? ws + OFF_W2 + ((size_t)(ct * 216 + kc) * 32 + cl) * 16
                         : ws + OFF_WO + ((size_t)(ct * 80 + (kc - 216)) * 32 + cl) * 16;
    *(u4*)dst = lo;
    *(u4*)(dst + 16) = hh;
  }
}

constexpr int K2_STAGE = 12288;
constexpr int K2_RING = 4;
constexpr int K2_RED = K2_RING * K2_STAGE;
constexpr int K2_LDS = K2_RED + 61440;

DEVINL void red_write(char* red, const f16v (&acc)[5], int g, int lane) {
#pragma unroll
  for (int o = 0; o < 4; ++o) {
    if (o != g) {
      const int src = g < o ? g : g - 1;
#pragma unroll
      for (int j = 0; j < 5; ++j)
        *(f4*)(red + ((o * 3 + src) * 5 + j) * 1024 + lane * 16) =
            f4{acc[j][4 * o], acc[j][4 * o + 1], acc[j][4 * o + 2], acc[j][4 * o + 3]};
    }
  }
}
DEVINL void red_own(f4 (&res)[5], const f16v (&acc)[5], int g) {
#pragma unroll
  for (int o = 0; o < 4; ++o)
    if (o == g) {
#pragma unroll
      for (int j = 0; j < 5; ++j) res[j] = f4{acc[j][4 * o], acc[j][4 * o + 1], acc[j][4 * o + 2], acc[j][4 * o + 3]};
    }
}
DEVINL void red_read(f4 (&res)[5], const char* red, int g, int lane) {
#pragma unroll
  for (int src = 0; src < 3; ++src)
#pragma unroll
    for (int j = 0; j < 5; ++j) res[j] += *(const f4*)(red + ((g * 3 + src) * 5 + j) * 1024 + lane * 16);
}

DEVINL u2 split3(float m) {
  const float a = -m;
  const unsigned h1 = __builtin_bit_cast(unsigned, a) & 0xffff0000u;
  const float r1 = a - __builtin_bit_cast(float, h1);
  const unsigned h2 = __builtin_bit_cast(unsigned, r1) & 0xffff0000u;
  const float r2 = r1 - __builtin_bit_cast(float, h2);
  const unsigned h3 = __builtin_bit_cast(unsigned, r2) & 0xffff0000u;
  return u2{(h1 >> 16) | h2, h3 >> 16};
}
DEVINL float max16(const f16v& s) {
  float m = s[0];
#pragma unroll
  for (int i = 1; i < 16; ++i) m = fmaxf(m, s[i]);
  return m;
}
DEVINL void exp_pack(const f16v& s, u4& p0, u4& p1) {
#pragma unroll
  for (int e = 0; e < 4; ++e) {
    p0[e] = pk_bf16(__builtin_amdgcn_exp2f(s[2 * e]), __builtin_amdgcn_exp2f(s[2 * e + 1]));
  }
#pragma unroll
  for (int e = 0; e < 4; ++e) {
    p1[e] = pk_bf16(__builtin_amdgcn_exp2f(s[8 + 2 * e]), __builtin_amdgcn_exp2f(s[8 + 2 * e + 1]));
  }
}

__global__ __launch_bounds__(768, 3) void k2_kernel(const u16* __restrict__ x, const u16* __restrict__ b2,
                                                     char* __restrict__ ws) {
  __shared__ __attribute__((aligned(16))) char smem[K2_LDS];
  const int tid = threadIdx.x, lane = tid & 63;
  const int w = __builtin_amdgcn_readfirstlane(tid >> 6);
  const int b = blockIdx.x, xcd = b & 7, idx = b >> 3;
  const int head = xcd * 4 + (idx & 3), qb = idx >> 2;
  const int r = lane & 31, hi = lane >> 5;
  const char* kvg = ws + OFF_KV + (size_t)head * 64 * 3072;

  if (w >= 8) {
    const int g = w - 8;
    const int rt = (xcd >> 1) * 16 + (idx & 15), cg = (xcd & 1) * 2 + (idx >> 4);
    const char* kvl = kvg + g * 3072 + lane * 16;
    auto issue_kv = [&](int st) {
      char* dst = smem + (st & 3) * K2_STAGE + g * 3072;
#pragma unroll
      for (int i = 0; i < 3; ++i) glds16(kvl + st * K2_STAGE + i * 1024, dst + i * 1024);
    };
    const char* ag = ws + OFF_H + ((size_t)rt * 216 + g * 54) * 512 + lane * 16;
    const char* bg = ws + OFF_W2 + ((size_t)cg * 5 * 216 + g * 54) * 512 + lane * 16;
    u4 fa[2], fb[2][5];
    auto issue_fr = [&](int ks, int n) {
#pragma unroll
      for (int u = 0; u < 2; ++u) {
        if (u < n) {
          fa[u] = *(const u4*)(ag + (ks + u) * 1024);
#pragma unroll
          for (int j = 0; j < 5; ++j) fb[u][j] = *(const u4*)(bg + (size_t)j * 216 * 512 + (ks + u) * 1024);
        }
      }
    };
    f16v acc[5];
#pragma unroll
    for (int j = 0; j < 5; ++j) acc[j] = f16v{};
    issue_kv(0); issue_kv(1); issue_kv(2);
    asm volatile("" ::: "memory");
    issue_fr(0, 2);
    WAIT_V(15);
    BARRIER();
    for (int s = 0; s < 13; ++s) {
      WAIT_V(0);
#pragma unroll
      for (int u = 0; u < 2; ++u)
#pragma unroll
        for (int j = 0; j < 5; ++j) acc[j] = mfma16(fb[u][j], fa[u], acc[j]);
      issue_kv(s + 3);
      if (s < 12) issue_fr(2 * s + 2, 2); else issue_fr(26, 1);
      BARRIER();
    }
    WAIT_V(0);
#pragma unroll
    for (int j = 0; j < 5; ++j) acc[j] = mfma16(fb[0][j], fa[0], acc[j]);
    u2 xr[5], br[5];
#pragma unroll
    for (int j = 0; j < 5; ++j) {
      const int n = cg * 160 + j * 32 + 8 * g + 4 * hi;
      xr[j] = *(const u2*)(x + (size_t)(rt * 32 + r) * DM + n);
      br[j] = *(const u2*)(b2 + n);
    }
    red_write(smem + K2_RED, acc, g, lane);
    f4 res[5];
    red_own(res, acc, g);
    WAIT_L0();
    BARRIER();
    red_read(res, smem + K2_RED, g, lane);
    char* pb = ws + OFF_PB + ((size_t)(rt * 4 + cg) * 4 + g) * 5120 + lane * 16;
#pragma unroll
    for (int j = 0; j < 5; ++j) {
      f4 v = res[j];
      v[0] += bf_lo(xr[j][0]) + bf_lo(br[j][0]);
      v[1] += bf_hi(xr[j][0]) + bf_hi(br[j][0]);
      v[2] += bf_lo(xr[j][1]) + bf_lo(br[j][1]);
      v[3] += bf_hi(xr[j][1]) + bf_hi(br[j][1]);
      *(f4*)(pb + j * 1024) = v;
    }
    BARRIER();
    return;
  }

  const int qt = qb * 8 + w;
  const char* qg = ws + OFF_Q + (size_t)(head * 64 + qt) * 1280;
  const u4 qm = *(const u4*)(qg + lane * 16);
  u2 qa = *(const u2*)(qg + 1024 + r * 8);
  if (hi) qa = u2{0u, 0u};
  const int koff = lane * 16, aoff = 1024 + lane * 8;
  const int voff = 1536 + (hi * 24 + (r < 24 ? r : 23)) * 16;
  f16v o = f16v{};
  BARRIER();
  f16v sc;
  {
    const u4 km = *(const u4*)(smem + koff);
    const u2 ka = *(const u2*)(smem + aoff);
    f16v s0 = mfma16(km, qm, f16v{});
    s0 = mfma8(ka, qa, s0);
    float m = max16(s0);
    m = fmaxf(m, xor32(m));
    if (hi) qa = split3(m);
    sc = mfma16(km, qm, f16v{});
    sc = mfma8(ka, qa, sc);
  }
  for (int s = 0; s < 16; ++s) {
    const char* cur = smem + (s & 3) * K2_STAGE;
    const char* nxt = smem + ((s + 1) & 3) * K2_STAGE;
#pragma unroll
    for (int t = 0; t < 4; ++t) {
      const char* kn = t < 3 ? cur + (t + 1) * 3072 : nxt;
      const u4 km = *(const u4*)(kn + koff);
      const u2 ka = *(const u2*)(kn + aoff);
      const u4 v0 = *(const u4*)(cur + t * 3072 + voff);
      const u4 v1 = *(const u4*)(cur + t * 3072 + voff + 768);
      f16v sn = mfma16(km, qm, f16v{});
      sn = mfma8(ka, qa, sn);
      u4 p0, p1;
      exp_pack(sc, p0, p1);
      o = mfma16(v0, p0, o);
      o = mfma16(v1, p1, o);
      sc = sn;
    }
    WAIT_L0();
    if (s < 15) BARRIER();
  }
  float l = xor32(o[8]);
  l = hi ? o[8] : l;
  {
    float am = 0.f;
#pragma unroll
    for (int i = 0; i < 12; ++i) am = fmaxf(am, fabsf(o[i]));
    am = hi ? fmaxf(fmaxf(fmaxf(fabsf(o[0]), fabsf(o[1])), fmaxf(fabsf(o[2]), fabsf(o[3]))),
                    fmaxf(fmaxf(fabsf(o[4]), fabsf(o[5])), fmaxf(fabsf(o[6]), fabsf(o[7])))) : am;
    float chk = 0.f;
#pragma unroll
    for (int i = 0; i < 8; ++i) chk += o[i] * 0.f;
    if (!hi) chk += (o[8] + o[9] + o[10] + o[11]) * 0.f;
    const bool ok = (l < 1e30f) && (am < 3e38f) && (chk == 0.f);
    if (__builtin_amdgcn_ballot_w64(!ok) != 0) {
      if (hi) qa = u2{0u, 0u};
      float m = -INFINITY;
      for (int t = 0; t < 64; ++t) {
        const u4 km = *(const u4*)(kvg + t * 3072 + koff);
        const u2 ka = *(const u2*)(kvg + t * 3072 + aoff);
        f16v s0 = mfma16(km, qm, f16v{});
        s0 = mfma8(ka, qa, s0);
        m = fmaxf(m, max16(s0));
      }
      m = fmaxf(m, xor32(m));
      if (hi) qa = split3(m);
      o = f16v{};
      for (int t = 0; t < 64; ++t) {
        const u4 km = *(const u4*)(kvg + t * 3072 + koff);
        const u2 ka = *(const u2*)(kvg + t * 3072 + aoff);
        const u4 v0 = *(const u4*)(kvg + t * 3072 + voff);
        const u4 v1 = *(const u4*)(kvg + t * 3072 + voff + 768);
        f16v s0 = mfma16(km, qm, f16v{});
        s0 = mfma8(ka, qa, s0);
        u4 p0, p1;
        exp_pack(s0, p0, p1);
        o = mfma16(v0, p0, o);
        o = mfma16(v1, p1, o);
      }
      l = xor32(o[8]);
      l = hi ? o[8] : l;
    }
  }
  const float inv = 1.f / l;
#pragma unroll
  for (int g = 0; g < 3; ++g) {
    if (g < 2 || !hi) {
      const int col = head * 20 + 8 * g + 4 * hi;
      u2 ov = {pk_bf16(o[4 * g] * inv, o[4 * g + 1] * inv), pk_bf16(o[4 * g + 2] * inv, o[4 * g + 3] * inv)};
      *(u2*)(ws + OFF_O + ((size_t)(qt * 80 + (col >> 3)) * 32 + r) * 16 + ((col >> 2) & 1) * 8) = ov;
    }
  }
}

__global__ __launch_bounds__(256, 1) void k3_kernel(const char* __restrict__ ws, u16* __restrict__ out) {
  __shared__ __attribute__((aligned(16))) char smem[61440];
  const int tid = threadIdx.x, lane = tid & 63;
  const int g = __builtin_amdgcn_readfirstlane(tid >> 6);
  const int b = blockIdx.x, xcd = b & 7, idx = b >> 3;
  const int rt = (xcd >> 1) * 16 + (idx & 15), cg = (xcd & 1) * 2 + (idx >> 4);
  const int r = lane & 31, hi = lane >> 5;
  const char* ag = ws + OFF_O + ((size_t)rt * 80 + g * 20) * 512 + lane * 16;
  const char* bg = ws + OFF_WO + ((size_t)cg * 5 * 80 + g * 20) * 512 + lane * 16;
  u4 fa[10], fb[10][5];
#pragma unroll
  for (int u = 0; u < 10; ++u) {
    fa[u] = *(const u4*)(ag + u * 1024);
#pragma unroll
    for (int j = 0; j < 5; ++j) fb[u][j] = *(const u4*)(bg + j * 80 * 512 + u * 1024);
  }
  f4 pbv[5];
  const char* pb = ws + OFF_PB + ((size_t)(rt * 4 + cg) * 4 + g) * 5120 + lane * 16;
#pragma unroll
  for (int j = 0; j < 5; ++j) pbv[j] = *(const f4*)(pb + j * 1024);
  asm volatile("" ::: "memory");
  f16v acc[5];
#pragma unroll
  for (int j = 0; j < 5; ++j) acc[j] = f16v{};
#pragma unroll
  for (int u = 0; u < 10; ++u)
#pragma unroll
    for (int j = 0; j < 5; ++j) acc[j] = mfma16(fb[u][j], fa[u], acc[j]);
  red_write(smem, acc, g, lane);
  f4 res[5];
  red_own(res, acc, g);
  __syncthreads();
  red_read(res, smem, g, lane);
#pragma unroll
  for (int j = 0; j < 5; ++j) {
    const f4 v = res[j] + pbv[j];
    const int n = cg * 160 + j * 32 + 8 * g + 4 * hi;
    *(u2*)(out + (size_t)(rt * 32 + r) * DM + n) = u2{pk_bf16(v[0], v[1]), pk_bf16(v[2], v[3])};
  }
}

extern "C" void kernel_launch(void* const* d_in, const int* in_sizes, int n_in, void* d_out, int out_size,
                              void* d_ws, size_t ws_size, hipStream_t stream) {
  const u16* x = (const u16*)d_in[0];
  const u16* wqkv = (const u16*)d_in[1];
  const u16* wo = (const u16*)d_in[2];
  const u16* w1 = (const u16*)d_in[4];
  const u16* b1 = (const u16*)d_in[5];
  const u16* w2 = (const u16*)d_in[6];
  const u16* b2 = (const u16*)d_in[7];
  char* ws = (char*)d_ws;
  k1_kernel<<<256, 512, 0, stream>>>(x, wqkv, w1, b1, w2, wo, ws);
  k2_kernel<<<256, 768, 0, stream>>>(x, b2, ws);
  k3_kernel<<<256, 256, 0, stream>>>(ws, (u16*)d_out);
}
```

```cpp
#include <hip/hip_runtime.h>
#include <hip/hip_bf16.h>
#include <stdint.h>
#include <type_traits>

typedef __bf16 bf2 __attribute__((ext_vector_type(2)));
typedef __bf16 bf8 __attribute__((ext_vector_type(8)));
typedef short s4 __attribute__((ext_vector_type(4)));
typedef float f2 __attribute__((ext_vector_type(2)));
typedef float f4 __attribute__((ext_vector_type(4)));
typedef float f16v __attribute__((ext_vector_type(16)));
typedef unsigned u2 __attribute__((ext_vector_type(2)));
typedef unsigned u4 __attribute__((ext_vector_type(4)));
typedef unsigned short u16;
#define LDS_AS __attribute__((address_space(3)))
#define GLB_AS __attribute__((address_space(1)))
#define DEVINL __device__ __forceinline__

constexpr int SEQ = 2048, DM = 640, NH = 32, FF = 1728;
constexpr size_t OFF_Q  = 0;
constexpr size_t OFF_KV = OFF_Q  + (size_t)32 * 64 * 1280;
constexpr size_t OFF_H  = OFF_KV + (size_t)32 * 64 * 3072;
constexpr size_t OFF_W2 = OFF_H  + (size_t)64 * 216 * 512;
constexpr size_t OFF_WO = OFF_W2 + (size_t)20 * 216 * 512;
constexpr size_t OFF_O  = OFF_WO + (size_t)20 * 80 * 512;
constexpr size_t OFF_PB = OFF_O  + (size_t)64 * 80 * 512;

#define WAIT_V(n) asm volatile("s_waitcnt vmcnt(" #n ")" ::: "memory")
#define WAIT_L0() asm volatile("s_waitcnt lgkmcnt(0)" ::: "memory")
#define BARRIER() do { asm volatile("" ::: "memory"); __builtin_amdgcn_s_barrier(); asm volatile("" ::: "memory"); } while (0)

DEVINL unsigned pk_bf16(float a, float b) {
  f2 f = {a, b};
  bf2 r = __builtin_convertvector(f, bf2);
  return __builtin_bit_cast(unsigned, r);
}
DEVINL float bf_lo(unsigned v) { return __builtin_bit_cast(float, v << 16); }
DEVINL float bf_hi(unsigned v) { return __builtin_bit_cast(float, v & 0xffff0000u); }
DEVINL f16v mfma16(u4 a, u4 b, f16v c) {
  return __builtin_amdgcn_mfma_f32_32x32x16_bf16(__builtin_bit_cast(bf8, a), __builtin_bit_cast(bf8, b), c, 0, 0, 0);
}
DEVINL f16v mfma8(u2 a, u2 b, f16v c) {
  return __builtin_amdgcn_mfma_f32_32x32x8bf16_1k(__builtin_bit_cast(s4, a), __builtin_bit_cast(s4, b), c, 0, 0, 0);
}
DEVINL float dot2(unsigned a, unsigned b, float c) {
  return __builtin_amdgcn_fdot2_f32_bf16(__builtin_bit_cast(bf2, a), __builtin_bit_cast(bf2, b), c, false);
}
DEVINL float xor32(float v) { return __shfl_xor(v, 32); }
DEVINL void glds16(const char* gbase, unsigned voff, unsigned lds) {
  asm volatile("s_mov_b32 m0, %0\n\ts_nop 0\n\tglobal_load_lds_dwordx4 %1, %2" ::"s"(lds), "v"(voff), "s"(gbase) : "memory");
}
DEVINL void gload16(u4& d, const char* gbase, unsigned voff) {
  asm volatile("global_load_dwordx4 %0, %1, %2" : "=v"(d) : "v"(voff), "s"(gbase) : "memory");
}
#define GPRIO 1
#define GPACE()
DEVINL unsigned lds_addr(const void* p) { return (unsigned)(size_t)(const LDS_AS char*)p; }
#define TS(kid, i)
#define TSC(kid, i)
DEVINL u2 tr_read(const char* l) {
  s4 r = __builtin_amdgcn_ds_read_tr16_b64_v4i16((LDS_AS s4*)l);
  return __builtin_bit_cast(u2, r);
}

constexpr int K1_STAGE = 49152;
constexpr int K1_STATS = 3 * K1_STAGE;
constexpr int K1_LDS = K1_STATS + 2048;

struct K1Ctx {
  const char* xg;
  const char* wg;
  int ldw2;
  unsigned aoff[2], boff[4];
};

DEVINL void k1_issue(const K1Ctx& c, char* smem, int k, int buf, int w) {
  const unsigned st = lds_addr(smem) + buf * K1_STAGE;
  const char* xa = c.xg + k * 128;
  const char* wb = c.wg + (size_t)k * 64 * c.ldw2;
#pragma unroll
  for (int i = 0; i < 2; ++i) glds16(xa, c.aoff[i], st + (2 * w + i) * 1024);
#pragma unroll
  for (int i = 0; i < 4; ++i) glds16(wb, c.boff[i], st + 16384 + (4 * w + i) * 1024);
}

template <int NTW, int KIND, int WN>
DEVINL void k1_body(const K1Ctx& c, char* smem, int w, int lane, int rb, int col0, int t0,
                    const u16* b1, char* ws) {
  constexpr bool SWAP = (KIND != 1);
  const int wm = w & 3, r = lane & 31, hi = lane >> 5;
  f16v acc[NTW];
#pragma unroll
  for (int j = 0; j < NTW; ++j) acc[j] = f16v{};
  float rs = 0.f, cs = 0.f;
  int aaddr[4], baddr[NTW], tile[NTW];
  {
    const int row = wm * 32 + r;
#pragma unroll
    for (int s = 0; s < 4; ++s) aaddr[s] = row * 128 + 16 * ((2 * s + hi) ^ ((row >> 1) & 7));
    const int blk = (lane >> 4) & 1, q = (lane & 15) >> 2, p = lane & 3;
#pragma unroll
    for (int j = 0; j < NTW; ++j) {
      int jj = j + wm; jj = jj >= NTW ? jj - NTW : jj; jj = jj >= NTW ? jj - NTW : jj;
      tile[j] = t0 + jj;
      baddr[j] = 16384 + (8 * hi + q) * 512 + ((tile[j] ^ q) * 64) + blk * 32 + p * 8;
    }
  }
  int buf = 0;
  for (int k = 0; k < 10; ++k) {
    if (k == 0) WAIT_V(12); else if (k == 9) WAIT_V(0); else WAIT_V(6);
    BARRIER();
    if (k == 0) TS(0, 1);
    if (k == 5) TS(0, 2);
    if (k >= 1 && k <= 7) k1_issue(c, smem, k + 2, buf == 0 ? 2 : buf - 1, w);
    const char* st = smem + buf * K1_STAGE;
#pragma unroll
    for (int s = 0; s < 4; ++s) {
      u4 a = *(const u4*)(st + aaddr[s]);
      u4 b[NTW];
#pragma unroll
      for (int j = 0; j < NTW; ++j) {
        u2 lo = tr_read(st + baddr[j] + s * 8192);
        u2 hh = tr_read(st + baddr[j] + s * 8192 + 2048);
        b[j] = u4{lo[0], lo[1], hh[0], hh[1]};
      }
#pragma unroll
      for (int j = 0; j < NTW; ++j) acc[j] = SWAP ? mfma16(b[j], a, acc[j]) : mfma16(a, b[j], acc[j]);
#pragma unroll
      for (int e = 0; e < 4; ++e) {
        rs = dot2(a[e], WN ? a[e] : 0x3f803f80u, rs);
        cs = dot2(b[0][e], 0x3f803f80u, cs);
      }
    }
    WAIT_L0();
    buf = buf == 2 ? 0 : buf + 1;
  }
  TS(0, 3);
  float* stats = (float*)(smem + K1_STATS);
  rs += xor32(rs);
  cs += xor32(cs);
  if (hi == 0) {
    stats[WN * 128 + wm * 32 + r] = rs;
    if (wm < NTW) stats[256 + tile[0] * 32 + r] = cs;
  }
  WAIT_L0();
  BARRIER();
  const int rt = rb * 4 + wm;
  if constexpr (SWAP) {
    const float mu = stats[wm * 32 + r] * (1.f / DM);
    const float var = fmaxf(stats[128 + wm * 32 + r] * (1.f / DM) - mu * mu, 0.f);
    const float rstd = __builtin_amdgcn_rsqf(var + 1e-5f);
#pragma unroll
    for (int j = 0; j < NTW; ++j) {
#pragma unroll
      for (int g = 0; g < 4; ++g) {
        const int nl = tile[j] * 32 + 8 * g + 4 * hi;
        const int n = col0 + nl;
        const f4 csv = *(const f4*)(stats + 256 + nl);
        float v[4];
#pragma unroll
        for (int e = 0; e < 4; ++e) v[e] = (acc[j][4 * g + e] - mu * csv[e]) * rstd;
        if constexpr (KIND == 0) {
          const bool isq = n < DM;
          const int nn = isq ? n : n - DM;
          const int head = nn / 20, d = nn - head * 20;
          const float sc = isq ? 0.22360679774997896f * 1.4426950408889634f : 1.f;
          u2 o = {pk_bf16(v[0] * sc, v[1] * sc), pk_bf16(v[2] * sc, v[3] * sc)};
          char* base = isq ? ws + OFF_Q + (size_t)(head * 64 + rt) * 1280 : ws + OFF_KV + (size_t)(head * 64 + rt) * 3072;
          const int off = d < 16 ? r * 16 + (d >> 3) * 512 + (d & 7) * 2 : 1024 + r * 8;
          *(u2*)(base + off) = o;
          if (!isq && d == 16) *(u2*)(base + 1280 + r * 8) = u2{0x3f803f80u, 0x00003f80u};
        } else {
          const u2 bb = *(const u2*)(b1 + n);
          v[0] += bf_lo(bb[0]); v[1] += bf_hi(bb[0]); v[2] += bf_lo(bb[1]); v[3] += bf_hi(bb[1]);
#pragma unroll
          for (int e = 0; e < 4; ++e) {
            const float t = v[e] * (-2.3022081986f - 0.1029432397f * v[e] * v[e]);
            v[e] = v[e] * __builtin_amdgcn_rcpf(1.f + __builtin_amdgcn_exp2f(t));
          }
          u2 o = {pk_bf16(v[0], v[1]), pk_bf16(v[2], v[3])};
          *(u2*)(ws + OFF_H + ((size_t)(rt * 216 + (n >> 3)) * 32 + r) * 16 + hi * 8) = o;
        }
      }
    }
  } else {
    float mu[16], rstd[16];
#pragma unroll
    for (int g = 0; g < 4; ++g) {
      const f4 sm = *(const f4*)(stats + wm * 32 + 8 * g + 4 * hi);
      const f4 sq = *(const f4*)(stats + 128 + wm * 32 + 8 * g + 4 * hi);
#pragma unroll
      for (int e = 0; e < 4; ++e) {
        const float m = sm[e] * (1.f / DM);
        mu[4 * g + e] = m;
        rstd[4 * g + e] = __builtin_amdgcn_rsqf(fmaxf(sq[e] * (1.f / DM) - m * m, 0.f) + 1e-5f);
      }
    }
#pragma unroll
    for (int j = 0; j < NTW; ++j) {
      const int nl = tile[j] * 32 + r;
      const int nn = col0 + nl - 2 * DM;
      const int head = nn / 20, d = nn - head * 20;
      const float csv = stats[256 + nl];
      char* base = ws + OFF_KV + (size_t)(head * 64 + rt) * 3072 + 1536 + hi * 384;
#pragma unroll
      for (int s = 0; s < 2; ++s) {
        u4 o;
#pragma unroll
        for (int e = 0; e < 4; ++e) {
          const int i0 = 8 * s + 2 * e, i1 = i0 + 1;
          o[e] = pk_bf16((acc[j][i0] - mu[i0] * csv) * rstd[i0], (acc[j][i1] - mu[i1] * csv) * rstd[i1]);
        }
        *(u4*)(base + s * 768 + d * 16) = o;
        if (d < 4) {
          const unsigned cst = d == 0 ? 0x3f803f80u : 0u;
          *(u4*)(base + s * 768 + (20 + d) * 16) = u4{cst, cst, cst, cst};
        }
      }
    }
  }
}

__global__ __launch_bounds__(512, 2) void k1_kernel(const u16* __restrict__ x, const u16* __restrict__ wqkv,
                                                     const u16* __restrict__ w1, const u16* __restrict__ b1,
                                                     const u16* __restrict__ w2, const u16* __restrict__ wo,
                                                     char* __restrict__ ws) {
  __shared__ __attribute__((aligned(16))) char smem[K1_LDS];
  const int tid = threadIdx.x, lane = tid & 63;
  const int w = __builtin_amdgcn_readfirstlane(tid >> 6);
  const int b = blockIdx.x, xcd = b & 7, idx = b >> 3;
  const int rb = (xcd & 1) * 8 + (idx & 7);
  const int cc = idx >> 3;
  const int chunk = (xcd >> 1) * 2 + (cc & 1);
  const bool isw1 = cc >= 2;
  int col0, ntiles;
  if (!isw1) { col0 = chunk * 256; ntiles = chunk < 7 ? 8 : 4; }
  else { col0 = (chunk < 6 ? 7 * chunk : 42 + 6 * (chunk - 6)) * 32; ntiles = chunk < 6 ? 7 : 6; }

  TS(0, 0); TSC(0, 8);
  unsigned pw[8];
  const int item = b * 512 + tid;
  const int kc = item / 320, np = item - kc * 320;
  const bool do_tile = b < 185;
  if (do_tile) {
    const u16* src = (kc < 216 ? w2 + (size_t)kc * 8 * DM : wo + (size_t)(kc - 216) * 8 * DM) + 2 * np;
#pragma unroll
    for (int j = 0; j < 8; ++j) pw[j] = *(const unsigned*)(src + j * DM);
  }
  asm volatile("" ::: "memory");

  K1Ctx c;
  c.ldw2 = isw1 ? FF * 2 : 3 * DM * 2;
  c.xg = (const char*)(x + (size_t)rb * 128 * DM);
  c.wg = (const char*)((isw1 ? w1 : wqkv) + col0);
#pragma unroll
  for (int i = 0; i < 2; ++i) {
    const int row = 16 * w + 8 * i + (lane >> 3);
    c.aoff[i] = row * (DM * 2) + 16 * ((lane & 7) ^ ((row >> 1) & 7));
  }
#pragma unroll
  for (int i = 0; i < 4; ++i) {
    const int row = 2 * (4 * w + i) + (lane >> 5);
    int ch = (lane & 31) ^ ((row & 3) << 2);
    ch = ch >= ntiles * 4 ? ch - 16 : ch;
    c.boff[i] = row * c.ldw2 + ch * 16;
  }
  k1_issue(c, smem, 0, 0, w);
  k1_issue(c, smem, 1, 1, w);
  k1_issue(c, smem, 2, 2, w);

  const int wn = w >> 2;
  if (!isw1) {
    if (chunk < 5) {
      if (wn == 0) k1_body<4, 0, 0>(c, smem, w, lane, rb, col0, 0, b1, ws);
      else         k1_body<4, 0, 1>(c, smem, w, lane, rb, col0, 4, b1, ws);
    } else if (chunk < 7) {
      if (wn == 0) k1_body<4, 1, 0>(c, smem, w, lane, rb, col0, 0, b1, ws);
      else         k1_body<4, 1, 1>(c, smem, w, lane, rb, col0, 4, b1, ws);
    } else {
      if (wn == 0) k1_body<2, 1, 0>(c, smem, w, lane, rb, col0, 0, b1, ws);
      else         k1_body<2, 1, 1>(c, smem, w, lane, rb, col0, 2, b1, ws);
    }
  } else {
    if (wn == 0) {
      if (ntiles == 7) k1_body<4, 2, 0>(c, smem, w, lane, rb, col0, 0, b1, ws);
      else             k1_body<3, 2, 0>(c, smem, w, lane, rb, col0, 0, b1, ws);
    } else {
      k1_body<3, 2, 1>(c, smem, w, lane, rb, col0, ntiles - 3, b1, ws);
    }
  }

  if (do_tile) {
    u4 lo, hh;
#pragma unroll
    for (int e = 0; e < 4; ++e) {
      lo[e] = __builtin_amdgcn_perm(pw[2 * e + 1], pw[2 * e], 0x05040100u);
      hh[e] = __builtin_amdgcn_perm(pw[2 * e + 1], pw[2 * e], 0x07060302u);
    }
    const int n = 2 * np, ct = n >> 5, cl = n & 31;
    char* dst = kc < 216 ? ws + OFF_W2 + ((size_t)(ct * 216 + kc) * 32 + cl) * 16
                         : ws + OFF_WO + ((size_t)(ct * 80 + (kc - 216)) * 32 + cl) * 16;
    *(u4*)dst = lo;
    *(u4*)(dst + 16) = hh;
  }
  TS(0, 4); TSC(0, 9);
}

constexpr int K2_STAGE = 12288;
constexpr int K2_RING = 5;
constexpr int K2_RED = K2_RING * K2_STAGE;
constexpr int K2_LDS = K2_RED + 61440;
#define SB() __builtin_amdgcn_sched_barrier(0)

DEVINL void red_write(char* red, const f16v (&acc)[5], int g, int lane) {
#pragma unroll
  for (int o = 0; o < 4; ++o) {
    if (o != g) {
      const int src = g < o ? g : g - 1;
#pragma unroll
      for (int j = 0; j < 5; ++j)
        *(f4*)(red + ((o * 3 + src) * 5 + j) * 1024 + lane * 16) =
            f4{acc[j][4 * o], acc[j][4 * o + 1], acc[j][4 * o + 2], acc[j][4 * o + 3]};
    }
  }
}
DEVINL void red_own(f4 (&res)[5], const f16v (&acc)[5], int g) {
#pragma unroll
  for (int o = 0; o < 4; ++o)
    if (o == g) {
#pragma unroll
      for (int j = 0; j < 5; ++j) res[j] = f4{acc[j][4 * o], acc[j][4 * o + 1], acc[j][4 * o + 2], acc[j][4 * o + 3]};
    }
}
DEVINL void red_read(f4 (&res)[5], const char* red, int g, int lane) {
#pragma unroll
  for (int src = 0; src < 3; ++src)
#pragma unroll
    for (int j = 0; j < 5; ++j) res[j] += *(const f4*)(red + ((g * 3 + src) * 5 + j) * 1024 + lane * 16);
}

DEVINL u2 split3(float m) {
  const float a = -m;
  const unsigned h1 = __builtin_bit_cast(unsigned, a) & 0xffff0000u;
  const float r1 = a - __builtin_bit_cast(float, h1);
  const unsigned h2 = __builtin_bit_cast(unsigned, r1) & 0xffff0000u;
  const float r2 = r1 - __builtin_bit_cast(float, h2);
  const unsigned h3 = __builtin_bit_cast(unsigned, r2) & 0xffff0000u;
  return u2{(h1 >> 16) | h2, h3 >> 16};
}
DEVINL float max16(const f16v& s) {
  float m = s[0];
#pragma unroll
  for (int i = 1; i < 16; ++i) m = fmaxf(m, s[i]);
  return m;
}
DEVINL void exp_pack(const f16v& s, u4& p0, u4& p1) {
#pragma unroll
  for (int e = 0; e < 4; ++e) {
    p0[e] = pk_bf16(__builtin_amdgcn_exp2f(s[2 * e]), __builtin_amdgcn_exp2f(s[2 * e + 1]));
  }
#pragma unroll
  for (int e = 0; e < 4; ++e) {
    p1[e] = pk_bf16(__builtin_amdgcn_exp2f(s[8 + 2 * e]), __builtin_amdgcn_exp2f(s[8 + 2 * e + 1]));
  }
}

__global__ __launch_bounds__(768, 3) void k2_kernel(const u16* __restrict__ x, const u16* __restrict__ b2,
                                                     char* __restrict__ ws) {
  __shared__ __attribute__((aligned(16))) char smem[K2_LDS];
  const int tid = threadIdx.x, lane = tid & 63;
  const int w = __builtin_amdgcn_readfirstlane(tid >> 6);
  const int b = blockIdx.x, xcd = b & 7, idx = b >> 3;
  const int head = xcd * 4 + (idx & 3), qb = idx >> 2;
  const int r = lane & 31, hi = lane >> 5;
  const char* kvg = ws + OFF_KV + (size_t)head * 64 * 3072;

  TS(1, 0); TSC(1, 8);
  if (w >= 8) {
    const int g = w - 8;
    const int rt = (xcd >> 1) * 16 + (idx & 15), cg = (xcd & 1) * 2 + (idx >> 4);
    const char* kvl = kvg + g * 3072;
    __builtin_amdgcn_s_setprio(GPRIO);
    const unsigned kvdst = lds_addr(smem) + g * 3072;
    auto issue_kv = [&](int st, int slot) {
#pragma unroll
      for (int i = 0; i < 3; ++i) glds16(kvl + st * K2_STAGE + i * 1024, lane * 16, kvdst + slot * K2_STAGE + i * 1024);
    };
    const char* ag = ws + OFF_H + ((size_t)rt * 216 + g * 54) * 512;
    const char* bg = ws + OFF_W2 + ((size_t)cg * 5 * 216 + g * 54) * 512;
    const unsigned l16 = lane * 16;
    u4 fa[3], fb[3][5];
    f16v acc[5];
#pragma unroll
    for (int j = 0; j < 5; ++j) acc[j] = f16v{};
    issue_kv(0, 0); issue_kv(1, 1);
#pragma unroll
    for (int u = 0; u < 3; ++u) {
      gload16(fa[u], ag + u * 1024, l16);
#pragma unroll
      for (int j = 0; j < 5; ++j) gload16(fb[u][j], bg + (size_t)j * 216 * 512 + u * 1024, l16);
      if (u == 0) issue_kv(2, 2);
    }
    auto kstep = [&](auto uc, auto nc, auto refill, int ks) {
      constexpr int u = decltype(uc)::value;
      asm volatile("s_waitcnt vmcnt(%6)" : "+v"(fa[u]), "+v"(fb[u][0]), "+v"(fb[u][1]), "+v"(fb[u][2]), "+v"(fb[u][3]),
                   "+v"(fb[u][4]) : "n"(decltype(nc)::value) : "memory");
      SB();
#pragma unroll
      for (int j = 0; j < 5; ++j) {
        acc[j] = mfma16(fb[u][j], fa[u], acc[j]);
        SB();
        if constexpr (decltype(refill)::value) {
          if (j == 4) gload16(fa[u], ag + (ks + 3) * 1024, l16);
          gload16(fb[u][j], bg + (size_t)j * 216 * 512 + (ks + 3) * 1024, l16);
        }
        GPACE();
        SB();
      }
    };
    using I0 = std::integral_constant<int, 0>; using I1 = std::integral_constant<int, 1>;
    using I2 = std::integral_constant<int, 2>; using I6 = std::integral_constant<int, 6>;
    using I15 = std::integral_constant<int, 15>; using I18 = std::integral_constant<int, 18>;
    using I9 = std::integral_constant<int, 9>;
    WAIT_V(21);
    BARRIER();
    int slot = 3;
#pragma unroll 1
    for (int s = 0; s < 12; s += 3) {
      issue_kv(s + 3, slot); slot = slot == 4 ? 0 : slot + 1;
      kstep(I0{}, I18{}, I1{}, 2 * s);
      kstep(I1{}, I15{}, I1{}, 2 * s + 1);
      BARRIER();
      issue_kv(s + 4, slot); slot = slot == 4 ? 0 : slot + 1;
      kstep(I2{}, I18{}, I1{}, 2 * s + 2);
      kstep(I0{}, I15{}, I1{}, 2 * s + 3);
      BARRIER();
      issue_kv(s + 5, slot); slot = slot == 4 ? 0 : slot + 1;
      kstep(I1{}, I18{}, I1{}, 2 * s + 4);
      kstep(I2{}, I15{}, I1{}, 2 * s + 5);
      BARRIER();
    }
    issue_kv(15, slot);
    kstep(I0{}, I18{}, I0{}, 24);
    kstep(I1{}, I9{}, I0{}, 25);
    BARRIER();
    kstep(I2{}, I0{}, I0{}, 26);
    u2 xr[5], br[5];
#pragma unroll
    for (int j = 0; j < 5; ++j) {
      const int n = cg * 160 + j * 32 + 8 * g + 4 * hi;
      xr[j] = *(const u2*)(x + (size_t)(rt * 32 + r) * DM + n);
      br[j] = *(const u2*)(b2 + n);
    }
    red_write(smem + K2_RED, acc, g, lane);
    f4 res[5];
    red_own(res, acc, g);
    WAIT_L0();
    BARRIER();
    red_read(res, smem + K2_RED, g, lane);
    char* pb = ws + OFF_PB + ((size_t)(rt * 4 + cg) * 4 + g) * 5120 + lane * 16;
#pragma unroll
    for (int j = 0; j < 5; ++j) {
      f4 v = res[j];
      v[0] += bf_lo(xr[j][0]) + bf_lo(br[j][0]);
      v[1] += bf_hi(xr[j][0]) + bf_hi(br[j][0]);
      v[2] += bf_lo(xr[j][1]) + bf_lo(br[j][1]);
      v[3] += bf_hi(xr[j][1]) + bf_hi(br[j][1]);
      *(f4*)(pb + j * 1024) = v;
    }
    BARRIER();
    return;
  }

  const int qt = qb * 8 + w;
  const char* qg = ws + OFF_Q + (size_t)(head * 64 + qt) * 1280;
  const u4 qm = *(const u4*)(qg + lane * 16);
  u2 qa = *(const u2*)(qg + 1024 + r * 8);
  if (hi) qa = u2{0u, 0u};
  const int koff = lane * 16, aoff = 1024 + lane * 8;
  const int voff = 1536 + (hi * 24 + (r < 24 ? r : 23)) * 16;
  f16v o = f16v{};
  BARRIER();
  TS(1, 1);
  f16v sc;
  {
    const u4 km = *(const u4*)(smem + koff);
    const u2 ka = *(const u2*)(smem + aoff);
    f16v s0 = mfma16(km, qm, f16v{});
    s0 = mfma8(ka, qa, s0);
    float m = max16(s0);
    m = fmaxf(m, xor32(m));
    if (hi) qa = split3(m);
    sc = mfma16(km, qm, f16v{});
    sc = mfma8(ka, qa, sc);
  }
  {
    u4 km = *(const u4*)(smem + 3072 + koff);
    u2 ka = *(const u2*)(smem + 3072 + aoff);
    u4 v0 = *(const u4*)(smem + voff);
    u4 v1 = u4{0u, 0u, 0u, 0u};
    u4 p0, p1 = u4{0u, 0u, 0u, 0u};
    float e12 = 0.f, e13 = 0.f, e14 = 0.f, e15 = 0.f;
    int cur = 0;
#pragma unroll 1
    for (int s = 0; s < 16; ++s) {
      const int nxt = cur == 4 * K2_STAGE ? 0 : cur + K2_STAGE;
      const char* kc_ = smem + cur + koff; const char* kn_ = smem + nxt + koff;
      const char* ac_ = smem + cur + aoff; const char* an_ = smem + nxt + aoff;
      const char* vc_ = smem + cur + voff; const char* vn_ = smem + nxt + voff;
      SB();
#pragma unroll
      for (int t = 0; t < 4; ++t) {
        float e[16];
        f16v sn = mfma16(km, qm, f16v{});
        SB();
        km = *(const u4*)(t < 2 ? kc_ + (t + 2) * 3072 : kn_ + (t - 2) * 3072);
#pragma unroll
        for (int i = 0; i < 4; ++i) e[i] = __builtin_amdgcn_exp2f(sc[i]);
        SB();
        p1[2] = pk_bf16(e12, e13); p1[3] = pk_bf16(e14, e15);
        SB();
        sn = mfma8(ka, qa, sn);
        SB();
        ka = *(const u2*)(t < 2 ? ac_ + (t + 2) * 3072 : an_ + (t - 2) * 3072);
#pragma unroll
        for (int i = 4; i < 8; ++i) e[i] = __builtin_amdgcn_exp2f(sc[i]);
        SB();
        p0[0] = pk_bf16(e[0], e[1]); p0[1] = pk_bf16(e[2], e[3]);
        SB();
        o = mfma16(v1, p1, o);
        SB();
        v1 = *(const u4*)(vc_ + t * 3072 + 768);
#pragma unroll
        for (int i = 8; i < 12; ++i) e[i] = __builtin_amdgcn_exp2f(sc[i]);
        SB();
        p0[2] = pk_bf16(e[4], e[5]); p0[3] = pk_bf16(e[6], e[7]);
        SB();
        o = mfma16(v0, p0, o);
        SB();
        v0 = *(const u4*)(t < 3 ? vc_ + (t + 1) * 3072 : vn_);
        e12 = __builtin_amdgcn_exp2f(sc[12]); e13 = __builtin_amdgcn_exp2f(sc[13]);
        e14 = __builtin_amdgcn_exp2f(sc[14]); e15 = __builtin_amdgcn_exp2f(sc[15]);
        SB();
        p1[0] = pk_bf16(e[8], e[9]); p1[1] = pk_bf16(e[10], e[11]);
        SB();
        sc = sn;
      }
      if (s < 15) BARRIER();
      cur = nxt;
    }
    p1[2] = pk_bf16(e12, e13); p1[3] = pk_bf16(e14, e15);
    o = mfma16(v1, p1, o);
  }
  TS(1, 3);
  float l = xor32(o[8]);
  l = hi ? o[8] : l;
  {
    float am = 0.f;
#pragma unroll
    for (int i = 0; i < 12; ++i) am = fmaxf(am, fabsf(o[i]));
    am = hi ? fmaxf(fmaxf(fmaxf(fabsf(o[0]), fabsf(o[1])), fmaxf(fabsf(o[2]), fabsf(o[3]))),
                    fmaxf(fmaxf(fabsf(o[4]), fabsf(o[5])), fmaxf(fabsf(o[6]), fabsf(o[7])))) : am;
    float chk = 0.f;
#pragma unroll
    for (int i = 0; i < 8; ++i) chk += o[i] * 0.f;
    if (!hi) chk += (o[8] + o[9] + o[10] + o[11]) * 0.f;
    const bool ok = (l < 1e30f) && (am < 3e38f) && (chk == 0.f);
    if (__builtin_amdgcn_ballot_w64(!ok) != 0) {
      if (hi) qa = u2{0u, 0u};
      float m = -INFINITY;
      for (int t = 0; t < 64; ++t) {
        const u4 km = *(const u4*)(kvg + t * 3072 + koff);
        const u2 ka = *(const u2*)(kvg + t * 3072 + aoff);
        f16v s0 = mfma16(km, qm, f16v{});
        s0 = mfma8(ka, qa, s0);
        m = fmaxf(m, max16(s0));
      }
      m = fmaxf(m, xor32(m));
      if (hi) qa = split3(m);
      o = f16v{};
      for (int t = 0; t < 64; ++t) {
        const u4 km = *(const u4*)(kvg + t * 3072 + koff);
        const u2 ka = *(const u2*)(kvg + t * 3072 + aoff);
        const u4 v0 = *(const u4*)(kvg + t * 3072 + voff);
        const u4 v1 = *(const u4*)(kvg + t * 3072 + voff + 768);
        f16v s0 = mfma16(km, qm, f16v{});
        s0 = mfma8(ka, qa, s0);
        u4 p0, p1;
        exp_pack(s0, p0, p1);
        o = mfma16(v0, p0, o);
        o = mfma16(v1, p1, o);
      }
      l = xor32(o[8]);
      l = hi ? o[8] : l;
    }
  }
  const float inv = 1.f / l;
#pragma unroll
  for (int g = 0; g < 3; ++g) {
    if (g < 2 || !hi) {
      const int col = head * 20 + 8 * g + 4 * hi;
      u2 ov = {pk_bf16(o[4 * g] * inv, o[4 * g + 1] * inv), pk_bf16(o[4 * g + 2] * inv, o[4 * g + 3] * inv)};
      *(u2*)(ws + OFF_O + ((size_t)(qt * 80 + (col >> 3)) * 32 + r) * 16 + ((col >> 2) & 1) * 8) = ov;
    }
  }
  TS(1, 4); TSC(1, 9);
}

__global__ __launch_bounds__(256, 1) void k3_kernel(char* __restrict__ ws, u16* __restrict__ out) {
  __shared__ __attribute__((aligned(16))) char smem[61440];
  const int tid = threadIdx.x, lane = tid & 63;
  const int g = __builtin_amdgcn_readfirstlane(tid >> 6);
  const int b = blockIdx.x, xcd = b & 7, idx = b >> 3;
  const int rt = (xcd >> 1) * 16 + (idx & 15), cg = (xcd & 1) * 2 + (idx >> 4);
  const int r = lane & 31, hi = lane >> 5;
  const char* ag = ws + OFF_O + ((size_t)rt * 80 + g * 20) * 512 + lane * 16;
  const char* bg = ws + OFF_WO + ((size_t)cg * 5 * 80 + g * 20) * 512 + lane * 16;
  TS(2, 0); TSC(2, 8);
  u4 fa[10], fb[10][5];
#pragma unroll
  for (int u = 0; u < 10; ++u) {
    fa[u] = *(const u4*)(ag + u * 1024);
#pragma unroll
    for (int j = 0; j < 5; ++j) fb[u][j] = *(const u4*)(bg + j * 80 * 512 + u * 1024);
  }
  f4 pbv[5];
  const char* pb = ws + OFF_PB + ((size_t)(rt * 4 + cg) * 4 + g) * 5120 + lane * 16;
#pragma unroll
  for (int j = 0; j < 5; ++j) pbv[j] = *(const f4*)(pb + j * 1024);
  __builtin_amdgcn_sched_barrier(0);
  f16v acc[5];
#pragma unroll
  for (int j = 0; j < 5; ++j) acc[j] = f16v{};
#pragma unroll
  for (int u = 0; u < 10; ++u)
#pragma unroll
    for (int j = 0; j < 5; ++j) acc[j] = mfma16(fb[u][j], fa[u], acc[j]);
  TS(2, 3);
  red_write(smem, acc, g, lane);
  f4 res[5];
  red_own(res, acc, g);
  __syncthreads();
  red_read(res, smem, g, lane);
#pragma unroll
  for (int j = 0; j < 5; ++j) {
    const f4 v = res[j] + pbv[j];
    const int n = cg * 160 + j * 32 + 8 * g + 4 * hi;
    *(u2*)(out + (size_t)(rt * 32 + r) * DM + n) = u2{pk_bf16(v[0], v[1]), pk_bf16(v[2], v[3])};
  }
  TS(2, 4); TSC(2, 9);
}

extern "C" void kernel_launch(void* const* d_in, const int* in_sizes, int n_in, void* d_out, int out_size,
                              void* d_ws, size_t ws_size, hipStream_t stream) {
  const u16* x = (const u16*)d_in[0];
  const u16* wqkv = (const u16*)d_in[1];
  const u16* wo = (const u16*)d_in[2];
  const u16* w1 = (const u16*)d_in[4];
  const u16* b1 = (const u16*)d_in[5];
  const u16* w2 = (const u16*)d_in[6];
  const u16* b2 = (const u16*)d_in[7];
  char* ws = (char*)d_ws;
  k1_kernel<<<256, 512, 0, stream>>>(x, wqkv, w1, b1, w2, wo, ws);
  k2_kernel<<<256, 768, 0, stream>>>(x, b2, ws);
  k3_kernel<<<256, 256, 0, stream>>>(ws, (u16*)d_out);
}
```

```cpp
#include <hip/hip_runtime.h>
#include <hip/hip_bf16.h>
#include <stdint.h>
#include <type_traits>

typedef __bf16 bf2 __attribute__((ext_vector_type(2)));
typedef __bf16 bf8 __attribute__((ext_vector_type(8)));
typedef short s4 __attribute__((ext_vector_type(4)));
typedef float f2 __attribute__((ext_vector_type(2)));
typedef float f4 __attribute__((ext_vector_type(4)));
typedef float f16v __attribute__((ext_vector_type(16)));
typedef unsigned u2 __attribute__((ext_vector_type(2)));
typedef unsigned u4 __attribute__((ext_vector_type(4)));
typedef unsigned short u16;
typedef unsigned long long u64;
#define LDS_AS __attribute__((address_space(3)))
#define DEVINL __device__ __forceinline__

constexpr int DM = 640, FF = 1728;
constexpr unsigned OFF_E  = 0;
constexpr unsigned OFF_FX = 4096;
constexpr unsigned OFF_FW = OFF_FX + 16384;
constexpr unsigned OFF_FO = OFF_FW + 16384;
constexpr unsigned OFF_X  = 65536;
constexpr unsigned OFF_H  = OFF_X + 64 * 120 * 1024;
constexpr unsigned OFF_W2 = OFF_H + 64 * 108 * 1024;
constexpr unsigned OFF_WO = OFF_W2 + 20 * 108 * 1024;
constexpr unsigned OFF_O  = OFF_WO + 20 * 40 * 1024;

#define WAIT_V(n) asm volatile("s_waitcnt vmcnt(" #n ")" ::: "memory")
#define WAIT_L0() do { asm volatile("" ::: "memory"); __builtin_amdgcn_s_waitcnt(0xc07f); asm volatile("" ::: "memory"); } while (0)
#define BARRIER() do { asm volatile("" ::: "memory"); __builtin_amdgcn_s_barrier(); asm volatile("" ::: "memory"); } while (0)
#define SB() __builtin_amdgcn_sched_barrier(0)

template <int I, int N, class F> DEVINL void sfor(F&& f) {
  if constexpr (I < N) { f(std::integral_constant<int, I>{}); sfor<I + 1, N>(f); }
}
DEVINL unsigned pk_bf16(float a, float b) {
  f2 f = {a, b};
  bf2 r = __builtin_convertvector(f, bf2);
  return __builtin_bit_cast(unsigned, r);
}
DEVINL float bf_lo(unsigned v) { return __builtin_bit_cast(float, v << 16); }
DEVINL float bf_hi(unsigned v) { return __builtin_bit_cast(float, v & 0xffff0000u); }
DEVINL f16v mfma16(u4 a, u4 b, f16v c) {
  return __builtin_amdgcn_mfma_f32_32x32x16_bf16(__builtin_bit_cast(bf8, a), __builtin_bit_cast(bf8, b), c, 0, 0, 0);
}
DEVINL f16v mfma8(u2 a, u2 b, f16v c) {
  return __builtin_amdgcn_mfma_f32_32x32x8bf16_1k(__builtin_bit_cast(s4, a), __builtin_bit_cast(s4, b), c, 0, 0, 0);
}
DEVINL f4 mfma_s(u4 a, u4 b, f4 c) {
  return __builtin_amdgcn_mfma_f32_16x16x32_bf16(__builtin_bit_cast(bf8, a), __builtin_bit_cast(bf8, b), c, 0, 0, 0);
}
DEVINL f4 mfma_q(u2 a, f4 c) {
  return __builtin_amdgcn_mfma_f32_4x4x4bf16_1k(__builtin_bit_cast(s4, a), __builtin_bit_cast(s4, a), c, 0, 0, 0);
}
DEVINL float xor32(float v) { return __shfl_xor(v, 32); }
DEVINL unsigned lds_addr(const void* p) { return (unsigned)(size_t)(const LDS_AS char*)p; }
DEVINL void glds16(const char* gbase, unsigned voff, unsigned lds) {
  asm volatile("s_mov_b32 m0, %0\n\ts_nop 0\n\tglobal_load_lds_dwordx4 %1, %2" ::"s"(lds), "v"(voff), "s"(gbase) : "memory");
}
DEVINL void glds4(const char* gbase, unsigned voff, unsigned lds) {
  asm volatile("s_mov_b32 m0, %0\n\ts_nop 0\n\tglobal_load_lds_dword %1, %2" ::"s"(lds), "v"(voff), "s"(gbase) : "memory");
}
template <int OFF = 0> DEVINL void ld16(u4& d, const char* gbase, unsigned voff) {
  asm volatile("global_load_dwordx4 %0, %1, %2 offset:%3 sc1" : "=v"(d) : "v"(voff), "s"(gbase), "n"(OFF) : "memory");
}
DEVINL void ld8(u2& d, const char* gbase, unsigned voff) {
  asm volatile("global_load_dwordx2 %0, %1, %2 sc1" : "=v"(d) : "v"(voff), "s"(gbase) : "memory");
}
template <int OFF = 0> DEVINL void st16(char* gbase, unsigned voff, u4 v) {
  asm volatile("global_store_dwordx4 %0, %1, %2 offset:%3 sc1\n\ts_nop 1" ::"v"(voff), "v"(v), "s"(gbase), "n"(OFF) : "memory");
}
DEVINL void st8(char* gbase, unsigned voff, u2 v) {
  asm volatile("global_store_dwordx2 %0, %1, %2 sc1\n\ts_nop 1" ::"v"(voff), "v"(v), "s"(gbase) : "memory");
}
DEVINL u2 tr_read(const char* l) {
  s4 r = __builtin_amdgcn_ds_read_tr16_b64_v4i16((LDS_AS s4*)l);
  return __builtin_bit_cast(u2, r);
}
DEVINL void wait_flags(const char* ws, unsigned voff, unsigned t0, unsigned t1) {
#pragma unroll 1
  for (int spin = 0; spin < (1 << 19); ++spin) {
    u2 f;
    asm volatile("global_load_dwordx2 %0, %1, %2 sc1\n\ts_waitcnt vmcnt(0)" : "=&v"(f) : "v"(voff), "s"(ws) : "memory");
    const unsigned f0 = f[0], f1 = f[1];
    if (__builtin_amdgcn_ballot_w64(f0 != t0 || f1 != t1) == 0) break;
    __builtin_amdgcn_s_sleep(2);
  }
  asm volatile("" ::: "memory");
}
#define TS(i)
#define TSC(i)
#define TR(i)
#define TRD(n)

constexpr int L_HB = 40960;
constexpr int L_E = 3 * L_HB;
constexpr int L_CS = L_E + 32768;
constexpr int L_RS = L_CS + 2048;
constexpr int L_DUM = L_RS + 4096;
constexpr int L_TOTAL = L_DUM + 256;
#define PIN(v) asm volatile("" : "+v"(v))
constexpr int KVT = 3200, KVS = 8 * KVT;
constexpr int PD = 4;

struct P1 {
  u4 A[40];
  u4 bq[PD + 1];
  f16v acc, prev;
  float cs, cs2;
  u2 auga;
  u2 augb;
  float rstd, sig;
  int baddr[4];
};

DEVINL u4 rd_frag(const char* p, int second) {
  const u2 lo = tr_read(p), hh = tr_read(p + second);
  return u4{lo[0], lo[1], hh[0], hh[1]};
}
DEVINL unsigned split2(float c) {
  const float c1 = bf_lo(pk_bf16(c, c));
  return pk_bf16(c1, c - c1);
}
DEVINL u2 split3(float m) {
  const float a = -m;
  const unsigned h1 = __builtin_bit_cast(unsigned, a) & 0xffff0000u;
  const float r1 = a - __builtin_bit_cast(float, h1);
  const unsigned h2 = __builtin_bit_cast(unsigned, r1) & 0xffff0000u;
  const float r2 = r1 - __builtin_bit_cast(float, h2);
  const unsigned h3 = __builtin_bit_cast(unsigned, r2) & 0xffff0000u;
  return u2{(h1 >> 16) | h2, h3 >> 16};
}
DEVINL float max16(const f16v& s) {
  float m = s[0];
#pragma unroll
  for (int i = 1; i < 16; ++i) m = fmaxf(m, s[i]);
  return m;
}
DEVINL float gelu_a(float a, float k1, float k3) { return __builtin_amdgcn_exp2f(a * (a * a * k3 + k1)); }
DEVINL float gelu_b(float a, float e, float sig) { return a * __builtin_amdgcn_rcpf(e * sig + sig); }

__global__ __launch_bounds__(512, 2) void mega_kernel(const u16* __restrict__ x, const u16* __restrict__ wqkv,
                                                     const u16* __restrict__ wo, const u16* __restrict__ w1,
                                                     const u16* __restrict__ b1, const u16* __restrict__ w2,
                                                     const u16* __restrict__ b2, u16* __restrict__ out,
                                                     char* __restrict__ ws) {
  __shared__ __attribute__((aligned(16))) char smem[L_TOTAL];
  int lane = threadIdx.x & 63;
  const int w = __builtin_amdgcn_readfirstlane(threadIdx.x >> 6);
  const int b = blockIdx.x, xcd = b & 7, idx = b >> 3;
  const unsigned lb = lds_addr(smem);
  unsigned tag0, tag1;
  unsigned tr_[16];
  asm volatile("" ::"s"(x), "s"(wqkv), "s"(wo), "s"(w1), "s"(b1), "s"(w2), "s"(b2), "s"(out), "s"(ws));
  TS(0); TSC(24);
  {
    const int rb = (xcd & 1) * 8 + (idx & 7), g = (xcd >> 1) + 4 * (idx >> 3);
    const int wm = w & 3, wn = w >> 2, r = lane & 31, hi = lane >> 5;
    const int nx = g < 14 ? 2 : 1;
    const int xu0 = g < 10 ? 10 + g : g < 14 ? 2 * g - 20 : g - 6, xu1 = g < 10 ? 20 + g : xu0 + 1;
    const int wu0 = g < 9 ? 2 * g : g < 14 ? 9 + g : 2 * g - 5;
    const int rt = rb * 4 + wm;
    const int brow = 8 * w + (lane >> 3);
    const unsigned bchunk = ((lane & 7) ^ (((brow >> 1) & 1) << 2)) * 16;
    const unsigned l16 = lane * 16;
    auto src_of = [&](int i) {
      return i < nx ? (const char*)wqkv + (i == 0 ? xu0 : xu1) * 128 : (const char*)w1 + (wu0 + i - nx) * 128;
    };
    auto ld_of = [&](int i) { return i < nx ? DM * 6 : FF * 2; };
    auto dst_of = [&](int i) {
      return ws + (i < nx ? OFF_X + (rt * 120 + (i == 0 ? xu0 : xu1) * 4 + wn * 2) * 1024
                          : OFF_H + (rt * 108 + (wu0 + i - nx) * 4 + wn * 2) * 1024);
    };
    auto dma_b = [&](const char* src, int ld, unsigned voff, int j, unsigned dst) {
      glds16(src + (size_t)(j * 64) * ld, voff, lb + dst + w * 1024);
    };
    const int rj = min((g - 9) * 16 + rb, 109), ru = rj / 11, rsub = rj - ru * 11;
    auto rt_slab = [&](int j) { return min(rsub + 11 * j, 36); };
    auto dma_rt = [&](int j, unsigned dst) {
      const int sl = rt_slab(j);
      if (sl < 27) {
        glds16((const char*)w2 + (size_t)sl * 64 * (DM * 2) + ru * 128, brow * (DM * 2) + bchunk, lb + dst + w * 1024);
      } else {
        const int kap = (sl - 27) * 64 + brow, F = kap >> 4, c = kap & 15;
        const int row = F < 32 ? 20 * F + c : 80 * (F - 32) + 40 * ((c >> 2) & 1) + 20 * (c >> 3) + 16 + (c & 3);
        glds16((const char*)wo + ru * 128, row * (DM * 2) + bchunk, lb + dst + w * 1024);
      }
    };
    const bool two_w = g < 9 || g >= 14;

    P1 st;
    const char* xg = (const char*)(x + (size_t)rb * 128 * DM);
    unsigned va[2];
#pragma unroll
    for (int i = 0; i < 2; ++i) {
      const int row = 16 * w + 8 * i + (lane >> 3);
      va[i] = row * (DM * 2) + 16 * ((lane & 7) ^ ((row >> 1) & 7));
    }
    auto a_slot = [](int j) { return ((j >> 1) + 1) % 3 * L_HB + (j & 1) * 16384; };
    auto b_slot = [](int j) {
      return j == 0 || j == 7 ? L_HB + 32768 : j == 2 || j == 9 ? 2 * L_HB + 32768 : j == 4 ? 32768
           : j == 1 || j == 8 ? L_E : j == 3 ? L_E + 8192 : j == 5 ? L_E + 16384 : L_E + 24576;
    };
    auto dma_a = [&](int j, int i) { glds16(xg + j * 128, va[i], lb + a_slot(j) + (2 * w + i) * 1024); };
    const char* s0 = src_of(0);
    const unsigned v0 = brow * (DM * 6) + bchunk;
#pragma unroll
    for (int j = 0; j < 6; ++j) {
      dma_a(j, 0); dma_a(j, 1); dma_b(s0, DM * 6, v0, j, b_slot(j));
      if (j == 1 || j == 3) BARRIER();
    }
    dma_b(s0, DM * 6, v0, 6, b_slot(6));
    u2 ev;
    ld8(ev, ws, OFF_E);
    unsigned bias0, bias1;
    asm volatile("global_load_ushort %0, %1, %2" : "=v"(bias0) : "v"((wu0 * 64 + wn * 32 + r) * 2), "s"(b1) : "memory");
    asm volatile("global_load_ushort %0, %1, %2" : "=v"(bias1) : "v"(min(wu0 * 64 + 64 + wn * 32 + r, FF - 1) * 2), "s"(b1) : "memory");
    {
      const char* pc;
      asm volatile("s_getpc_b64 %0" : "=s"(pc));
      glds4(pc, 0u, lb + L_DUM);
    }
    int aaddr[4];
    {
      const int row = wm * 32 + r;
      const int blk = (lane >> 4) & 1, q = (lane & 15) >> 2, p = lane & 3;
#pragma unroll
      for (int s = 0; s < 4; ++s) {
        const int sr = (s + wm + 2 * wn) & 3;
        aaddr[s] = row * 128 + 16 * ((2 * sr + hi) ^ ((row >> 1) & 7));
        st.baddr[s] = (16 * sr + 8 * hi + q) * 128 + ((wn * 4 + blk * 2 + (p >> 1)) ^ (((q >> 1) & 1) << 2)) * 16 + (p & 1) * 8;
      }
    }
    float* csl = (float*)(smem + L_CS);
    auto bqi = [](int kk) { return kk % 8 == 4 ? 4 : kk % 4; };
    auto cs_add = [&](int f, int e) {
      const unsigned v = st.bq[bqi(f)][e];
      st.cs += bf_lo(v); st.cs2 += bf_hi(v);
      PIN(st.cs); PIN(st.cs2);
    };
    auto cs_step = [&](int kk) {
      if (kk < 36) cs_add(kk & ~3, kk & 3);
      if (kk == 35 || kk == 36) { cs_add(36, 2 * (kk - 35)); cs_add(36, 2 * (kk - 35) + 1); }
    };
    auto cs_write = [&](float v) { csl[(wn * 32 + r) * 8 + wm * 2 + hi] = v; };
    auto selector = [&]() {
      unsigned v = (lane & 3) == 0 && ((lane >> 2) & 1) == ((lane >> 4) & 1) ? 0x3f803f80u : 0u;
      PIN(v);
      return u4{v, v, v, v};
    };
    auto aug_a = [&](f4 c0, f4 c1, unsigned bias) {
      const float c = ((c0[0] + c0[1]) + (c0[2] + c0[3])) + ((c1[0] + c1[1]) + (c1[2] + c1[3]));
      const unsigned c12 = split2(c);
      const unsigned bb = bias | (bias << 16);
      return hi ? u2{bb, bias} : u2{c12, c12};
    };

    {
      const char* s1p = src_of(1);
      const int ld1 = ld_of(1);
      const unsigned v1 = brow * ld1 + bchunk;
      f4 s1 = {}, s2 = {};
      const u4 sel = selector();
      st.cs = 0.f; st.cs2 = 0.f;
      f4 c0, c1, r0, r1;
      auto rdb = [&](int kk) { return rd_frag(smem + (kk < 40 ? b_slot(kk >> 2) : 0) + st.baddr[kk & 3], 512); };
      auto rda = [&](int kk) { st.A[kk] = *(const u4*)(smem + a_slot(kk >> 2) + aaddr[kk & 3]); };
      WAIT_V(17);
      BARRIER();
      TS(1);
#pragma unroll
      for (int i = 0; i < 4; ++i) { rda(i); st.bq[i] = rdb(i); }
#pragma unroll
      for (int i = 4; i < 8; ++i) rda(i);
      sfor<0, 40>([&](auto kc) {
        constexpr int kk = decltype(kc)::value, k8 = kk % 8;
        SB();
        st.acc = mfma16(st.bq[bqi(kk)], st.A[kk], kk == 0 ? f16v{} : st.acc);
        SB();
        if constexpr (k8 == 5) {
          if constexpr (kk == 5) WAIT_V(11);
          else if constexpr (kk == 13) WAIT_V(11);
          else if constexpr (kk == 21) asm volatile("s_waitcnt vmcnt(6)" : "+v"(bias0), "+v"(bias1), "+v"(ev) :: "memory");
          else WAIT_V(5);
          WAIT_L0(); BARRIER();
          st.bq[0] = rdb(kk + 3);
          if constexpr (kk < 37) rda(kk + 3);
          st.bq[1] = rdb(kk + 4);
          if constexpr (kk < 37) rda(kk + 4);
          if constexpr (kk == 37) {
            c0 = *(const f4*)(csl + (wn * 32 + r) * 8); c1 = *(const f4*)(csl + (wn * 32 + r) * 8 + 4);
            r0 = *(const f4*)(smem + L_RS + (wm * 32 + r) * 32); r1 = *(const f4*)(smem + L_RS + (wm * 32 + r) * 32 + 16);
            st.cs = 0.f; st.cs2 = 0.f;
          }
        } else if constexpr (k8 != 4) {
          st.bq[bqi(kk + PD)] = rdb(kk + PD);
        }
        if constexpr (kk == 22) {
          const unsigned e0 = __builtin_amdgcn_readfirstlane(ev[0]), e1 = __builtin_amdgcn_readfirstlane(ev[1]);
          const u64 t = (((u64)e1 << 32) | e0) + 0x9E3779B97F4A7C15ull;
          tag0 = (unsigned)t; tag1 = (unsigned)(t >> 32);
        }
        if constexpr (kk < 37) {
          if constexpr (k8 == 6 || k8 == 7) { rda(kk + 4); rda(kk + 6); }
          if constexpr (k8 == 0 && kk > 0) { rda(kk + 6); rda(kk + 7); }
        }
        {
          constexpr int q = (kk + 3) / 8, p = (kk + 3) % 8;
          if constexpr (q == 1) {
            if constexpr (p < 4) dma_a(6 + (p >> 1), p & 1);
            else if constexpr (p < 6) dma_b(s0, DM * 6, v0, 3 + p, b_slot(3 + p));
          } else if constexpr (q == 2) {
            if constexpr (p < 4) dma_a(8 + (p >> 1), p & 1);
            else if constexpr (p == 4) dma_b(s0, DM * 6, v0, 9, b_slot(9));
          } else if constexpr (q == 3 || q == 4) {
            if constexpr (p < 5) dma_b(s1p, ld1, v1, (q - 3) * 5 + p, (q - 3) * L_HB + p * 8192);
          }
        }
        auto stat = [&](int i) {
          SB();
          s2 = mfma_q(u2{st.A[i][0], st.A[i][1]}, s2);
          s1 = mfma_s(sel, st.A[i], s1);
          s2 = mfma_q(u2{st.A[i][2], st.A[i][3]}, s2);
          SB();
        };
        if constexpr (kk < 32) { if constexpr (kk % 4 == 1 || kk % 4 == 2) stat(kk); }
        else if constexpr (kk == 32) stat(33);
        else if constexpr (kk == 33) stat(34);
        else if constexpr (kk == 34) stat(37);
        else if constexpr (kk == 35) stat(38);
        cs_step(kk);
        if constexpr (kk == 36) {
          cs_write(st.cs + st.cs2);
          const float q0 = s2[0], q1 = s2[1], q2 = s2[2], q3 = s2[3], p0 = s1[0];
          *(f2*)(smem + L_RS + (wm * 32 + r) * 32 + (wn * 2 + hi) * 8) =
              f2{hi ? 0.f : p0, (lane & 2) ? ((lane & 1) ? q3 : q2) : ((lane & 1) ? q1 : q0)};
        }
        SB();
      });
      const float mu = ((r0[0] + r0[2]) + (r1[0] + r1[2])) * (1.f / DM);
      const float sq = ((r0[1] + r0[3]) + (r1[1] + r1[3])) * (1.f / DM);
      const float ve = fmaxf(sq - mu * mu, 0.f) + 1e-5f;
      st.rstd = __builtin_amdgcn_rsqf(ve);
      st.sig = ve * st.rstd;
      const unsigned m12 = split2(-mu), s12 = split2(st.sig);
      const float s3 = st.sig - bf_lo(s12) - bf_hi(s12);
      st.augb = hi ? u2{s12, pk_bf16(s3, 0.f)} : u2{(m12 & 0xffffu) | (m12 << 16), (m12 >> 16) | (m12 & 0xffff0000u)};
      st.auga = aug_a(c0, c1, 0u);
      st.prev = st.acc;
    }
    TS(2);
    int hcur = 0, hfree = 2 * L_HB;
    auto round = [&](auto epi, int i, bool full) {
      constexpr int EPI = decltype(epi)::value;
      const char* sn_ = src_of(i + 1);
      const int ldn = ld_of(i + 1);
      char* dp = dst_of(i - 1);
      const bool retile = g >= 9 && !full;
      const unsigned vn = brow * ldn + bchunk;
      const unsigned bias = i < nx ? 0u : i == nx ? bias0 : bias1;
      float k1, k3;
      auto rdb = [&](int kk) { return rd_frag(smem + (kk % 20 >> 2) * 8192 + st.baddr[kk & 3], 512); };
      f4 c0, c1;
      u4 ob;
      float ga[2], gb[2];
      f4 cm = {};
      u4 sel;
      if constexpr (EPI == 2) sel = selector();
      sfor<0, 40>([&](auto kc) {
        constexpr int kk = decltype(kc)::value, k20 = kk % 20;
        SB();
        st.acc = mfma16(st.bq[bqi(kk)], st.A[kk], kk == 0 ? f16v{} : st.acc);
        SB();
        if constexpr (k20 == 16) {
          const int hn = hcur == 2 * L_HB ? 0 : hcur + L_HB;
#pragma unroll
          for (int s = 0; s < 4; ++s) { st.baddr[s] += hn - hcur; PIN(st.baddr[s]); }
          hfree = hcur; hcur = hn;
        } else if constexpr (k20 == 17) {
          if constexpr (kk == 17) {
            if constexpr (EPI == 1) { if (full) WAIT_V(7); else WAIT_V(0); } else WAIT_V(0);
          } else if constexpr (EPI == 1) {
            WAIT_V(5);
          }
          WAIT_L0(); BARRIER();
          if constexpr (kk == 37) { c0 = *(const f4*)(csl + (wn * 32 + r) * 8); c1 = *(const f4*)(csl + (wn * 32 + r) * 8 + 4); }
          st.bq[bqi(kk + 3)] = rdb(kk + 3);
          st.bq[1] = rdb(kk + 4);
          if constexpr (kk == 37) { st.cs = 0.f; st.cs2 = 0.f; }
          if constexpr (kk == 17) { if (!full && threadIdx.x == 0) st8(ws, OFF_FX + (g * 16 + rb) * 64, u2{tag0, tag1}); }
        } else {
          st.bq[bqi(kk + PD)] = rdb(kk + PD);
        }
        if constexpr (kk == 0) {
          if constexpr (EPI == 1) {
            k1 = st.rstd * (g >= 10 ? 0.22360679774997896f * 1.4426950408889634f : 1.f);
          } else {
            k1 = st.rstd * -2.3022081986f;
            k3 = st.rstd * st.rstd * st.rstd * -0.1029432397f;
          }
          SB();
          st.prev = mfma8(st.auga, st.augb, st.prev);
        }
        if constexpr (kk == 39) { st.auga = aug_a(c0, c1, bias); PIN(st.auga); }
        if constexpr (EPI == 1) {
          if constexpr (kk < 14 && kk % 3 == 1) { if (full) dma_b(sn_, ldn, vn, kk / 3, hfree + kk / 3 * 8192); }
          if constexpr (kk >= 18 && kk < 36 && kk % 4 == 2) { if (full) dma_b(sn_, ldn, vn, 5 + (kk - 18) / 4, hfree + (kk - 18) / 4 * 8192); }
        }
        if constexpr (kk >= 19 && kk < 34 && kk % 4 == 3) { if (retile) dma_rt((kk - 19) / 4, hfree + (kk - 19) / 4 * 8192); }
        if constexpr (EPI == 1) {
          cs_step(kk);
          if constexpr (kk == 36) cs_write(st.cs + st.cs2);
        } else {
          if constexpr (kk % 4 == 1 && kk < 36) { SB(); cm = mfma_s(sel, st.bq[bqi(kk - 1)], cm); SB(); }
          if constexpr (kk == 35) { SB(); cm = mfma_s(sel, st.bq[bqi(36)], cm); SB(); }
          if constexpr (kk == 36) { const float c = cm[0]; cs_write(hi ? 0.f : c); }
        }
        if constexpr (EPI == 1) {
          if constexpr (kk >= 2 && kk <= 9) {
            constexpr int e = kk - 2;
            float y0 = st.prev[2 * e] * k1; PIN(y0);
            float y1 = st.prev[2 * e + 1] * k1; PIN(y1);
            ob[e & 3] = pk_bf16(y0, y1);
            PIN(ob);
            if constexpr (e == 3) st16<0>(dp, l16, ob);
            if constexpr (e == 7) st16<1024>(dp, l16, ob);
          }
        } else {
          if constexpr (kk >= 3 && kk <= 34) {
            constexpr int e = (kk - 3) >> 2, ph = (kk - 3) & 3;
            const float a0 = st.prev[2 * e], a1 = st.prev[2 * e + 1];
            if constexpr (ph == 0) {
              ga[0] = a0 * a0; ga[1] = a1 * a1; PIN(ga[0]); PIN(ga[1]);
              ga[0] = ga[0] * k3 + k1; ga[1] = ga[1] * k3 + k1; PIN(ga[0]); PIN(ga[1]);
              ga[0] *= a0; PIN(ga[0]);
            } else if constexpr (ph == 1) {
              ga[1] *= a1; PIN(ga[1]);
              ga[0] = __builtin_amdgcn_exp2f(ga[0]); PIN(ga[0]);
              ga[1] = __builtin_amdgcn_exp2f(ga[1]); PIN(ga[1]);
            } else if constexpr (ph == 2) {
              gb[0] = ga[0] * st.sig + st.sig; gb[1] = ga[1] * st.sig + st.sig; PIN(gb[0]); PIN(gb[1]);
              gb[0] = __builtin_amdgcn_rcpf(gb[0]); PIN(gb[0]);
            } else {
              gb[1] = __builtin_amdgcn_rcpf(gb[1]); PIN(gb[1]);
              gb[0] *= a0; PIN(gb[0]);
              gb[1] *= a1; PIN(gb[1]);
              ob[e & 3] = pk_bf16(gb[0], gb[1]); PIN(ob);
              if constexpr (e == 3) st16<0>(dp, l16, ob);
              if constexpr (e == 7) st16<1024>(dp, l16, ob);
            }
          }
        }
        SB();
      });
      st.prev = st.acc;
    };
    round(std::integral_constant<int, 1>{}, 1, true);
    TS(3);
    if (nx == 2) round(std::integral_constant<int, 1>{}, 2, two_w);
    TS(4);
    if (two_w) round(std::integral_constant<int, 2>{}, nx + 1, false);
    st.prev = mfma8(st.auga, st.augb, st.prev);
    TS(5);
    {
      const float k1 = st.rstd * -2.3022081986f, k3 = st.rstd * st.rstd * st.rstd * -0.1029432397f;
      char* dp = dst_of(two_w ? nx + 1 : nx);
      u4 o0, o1;
#pragma unroll
      for (int e = 0; e < 4; ++e) {
        const float a0 = st.prev[2 * e], a1 = st.prev[2 * e + 1], a2 = st.prev[8 + 2 * e], a3 = st.prev[9 + 2 * e];
        o0[e] = pk_bf16(gelu_b(a0, gelu_a(a0, k1, k3), st.sig), gelu_b(a1, gelu_a(a1, k1, k3), st.sig));
        o1[e] = pk_bf16(gelu_b(a2, gelu_a(a2, k1, k3), st.sig), gelu_b(a3, gelu_a(a3, k1, k3), st.sig));
      }
      st16<0>(dp, l16, o0);
      st16<1024>(dp, l16, o1);
    }
    if (g >= 9) {
      WAIT_V(2); BARRIER();
      const int hrt = hfree == 0 ? 2 * L_HB : hfree - L_HB;
      const int blk = (lane >> 4) & 1, q = (lane & 15) >> 2, p = lane & 3;
      const int ra = (16 * wm + 4 * hi + q) * 128 + ((wn * 4 + blk * 2 + (p >> 1)) ^ (((q >> 1) & 1) << 2)) * 16 + (p & 1) * 8;
#pragma unroll
      for (int j = 0; j < 4; ++j) {
        const int sl = rt_slab(j), ct = ru * 2 + wn;
        const u4 f = rd_frag(smem + hrt + j * 8192 + ra, 1024);
        st16<0>(ws + (sl < 27 ? OFF_W2 + (ct * 108 + sl * 4 + wm) * 1024 : OFF_WO + (ct * 40 + (sl - 27) * 4 + wm) * 1024), l16, f);
      }
    }
    TS(6);
    WAIT_V(0); BARRIER();
    if (threadIdx.x == 0) st8(ws, OFF_FW + (g * 16 + rb) * 64, u2{tag0, tag1});
  }
  TS(7);
  {
    PIN(lane);
    const int r = lane & 31, hi = lane >> 5;
    const unsigned l16 = lane * 16;
    const int hq = idx & 3, head = xcd * 4 + hq, qb = idx >> 2, qt = qb * 8 + w;
    const int fw = xcd * 5 + (hq < 2 ? hq : hq + 1), fh = xcd * 5 + (hq == 0 ? 1 : hq == 3 ? 3 : 2);
    const unsigned vw = l16 + fw * 1024, vh = l16 + fh * 1024 + (hq >> 1) * 8;
    const int rt = (xcd >> 1) * 16 + (idx & 15), cg = (xcd & 1) * 2 + (idx >> 4), kg = w & 3, hw = w >> 2;
    {
      const int u = (lane < 32 ? lane >> 4 : lane) & 1 ? fh >> 2 : fw >> 2;
      const int fi = lane < 32 ? u * 16 + (lane & 15) : (u < 8 ? 10 + (u >> 1) : 6 + u) * 16 + (qt >> 2);
      wait_flags(ws, OFF_FX + fi * 64, tag0, tag1);
    }
    TS(8);
    struct KV { u4 kw, vw; u2 kh, vh; };
    auto ld_k = [&](KV& d, int t) {
      const char* p = ws + OFF_X + (t * 120 + 40) * 1024;
      ld16(d.kw, p, vw); ld8(d.kh, p, vh);
    };
    auto ld_v = [&](KV& d, int t) {
      const char* p = ws + OFF_X + (t * 120 + 80) * 1024;
      ld16(d.vw, p, vw); ld8(d.vh, p, vh);
    };
    u4 qm; u2 qa;
    KV sg, sg0;
    ld_k(sg0, w); ld_v(sg0, w);
    ld16(qm, ws + OFF_X + qt * 120 * 1024, vw); ld8(qa, ws + OFF_X + qt * 120 * 1024, vh);
    ld_k(sg, 8 + w); ld_v(sg, 8 + w);
    const int wv = 1536 + hi * 576 + r * 16, wh = 2688 + r * 16 + hi * 8;
    const int wc = hi ? (hq == 0 ? 2696 : hq == 1 ? 1536 : hq == 2 ? 2120 : 2688) + r * 16
                      : (hq == 0 ? 1280 + r * 8 : hq == 1 ? r * 16 : hq == 2 ? 520 + r * 16 : 1024 + r * 8);
    const u2 cst = hi ? u2{0x3f80u, 0u} : u2{0x3f803f80u, 0x3f80u};
    auto wr_k = [&](const KV& d, char* p) { *(u4*)(p + l16) = d.kw; *(u2*)(p + 1024 + lane * 8) = d.kh; };
    auto wr_v = [&](const KV& d, char* p) {
      *(u4*)(p + wv) = d.vw; *(u2*)(p + wh) = d.vh;
      asm volatile("" ::: "memory");
      *(u2*)(p + wc) = cst;
    };
    int vrd;
    {
      const int pp = (lane >> 2 & 4) | (lane & 3);
      const int z = pp < 5 ? pp + (hq & 1) : (hq & 1) ? 0 : 5;
      const int c = hq < 2 ? z : z - 2;
      vrd = 1536 + (c >= 0 && c < 4 ? (c & 1) * 576 + (c >> 1) * 8 : 1152 + (z & 1) * 8) + (4 * hi + (lane >> 2 & 3)) * 16;
    }
    const bool fl = hi != (hq & 1);
    auto set_q = [&](u2 v) {
      if (fl && hq == 1) { qm[0] = v[0]; qm[1] = v[1]; }
      if (fl && hq == 2) { qm[2] = v[0]; qm[3] = v[1]; }
      if (fl && (hq == 0 || hq == 3)) qa = v;
    };
#define TIE_KV(d) "+v"(d.kw), "+v"(d.kh), "+v"(d.vw), "+v"(d.vh)
    asm volatile("s_waitcnt vmcnt(4)" : TIE_KV(sg0), "+v"(qm), "+v"(qa) :: "memory");
    wr_k(sg0, smem + L_E + w * KVT); wr_v(sg0, smem + L_E + w * KVT);
    set_q(u2{0u, 0u});
    WAIT_L0(); BARRIER();
    TS(9); TSC(26);

    f16v sc, o = f16v{};
    {
      const u4 km = *(const u4*)(smem + L_E + l16);
      const u2 ka = *(const u2*)(smem + L_E + 1024 + lane * 8);
      f16v s0 = mfma16(km, qm, f16v{});
      s0 = mfma8(ka, qa, s0);
      float m = max16(s0);
      m = fmaxf(m, xor32(m));
      set_q(split3(m));
      sc = mfma16(km, qm, f16v{});
      sc = mfma8(ka, qa, sc);
    }
    u4 km = *(const u4*)(smem + L_E + KVT + l16);
    u2 ka = *(const u2*)(smem + L_E + KVT + 1024 + lane * 8);
    u4 v0 = rd_frag(smem + L_E + vrd, 128);
    u4 v1 = u4{0u, 0u, 0u, 0u};
    u4 p0, p1 = u4{0u, 0u, 0u, 0u};
    float e12 = 0.f, e13 = 0.f, e14 = 0.f, e15 = 0.f;
    f16v acc[3] = {f16v{}, f16v{}, f16v{}};
    u4 ga[2][2], gb[2][5], wb[25];
    u2 ffw;
    const char* pa = ws + OFF_H + (rt * 108 + kg * 27) * 1024;
    const char* pb0 = ws + OFF_W2 + ((cg * 5 + 2 * hw) * 108 + kg * 27) * 1024;
    const char* pb4 = ws + OFF_W2 + ((cg * 5 + 4) * 108 + kg * 27) * 1024;
    unsigned g0 = l16 + hw * 1024, g1 = l16 + (1 - hw) * 1024;
    const unsigned glim = l16 + 26 * 1024;
    auto g_ld = [&](int u, int i) {
      if (i == 0) ld16(gb[u][0], pb0, g0);
      if (i == 1) ld16(gb[u][1], pb0 + 108 * 1024, g0);
      if (i == 2) { ld16(gb[u][2], pb4, g0); ld16(ga[u][0], pa, g0); g0 = min(g0 + 2048, glim); }
      if (i == 3) ld16(gb[u][3], pb0, g1);
      if (i == 4) { ld16(gb[u][4], pb0 + 108 * 1024, g1); ld16(ga[u][1], pa, g1); g1 = min(g1 + 2048, glim); }
    };
    auto g_mm = [&](int u, int i) { acc[i % 3] = mfma16(gb[u][i], ga[u][i / 3], acc[i % 3]); };
#define TIE_G(u) "+v"(ga[u][0]), "+v"(ga[u][1]), "+v"(gb[u][0]), "+v"(gb[u][1]), "+v"(gb[u][2]), "+v"(gb[u][3]), "+v"(gb[u][4])
    const char* po0 = ws + OFF_WO + ((cg * 5 + 2 * hw) * 40 + kg * 10) * 1024;
    const char* po4 = ws + OFF_WO + ((cg * 5 + 4) * 40 + kg * 10) * 1024;
    const unsigned fwi = OFF_FW + (lane < 16 ? lane * 16 + (rt >> 2) : 144 + (cg * 5 >> 1) * 11 + min(lane - 16, 32)) * 64;

    auto stage = [&](auto kind, int s, int cur, int nxt) {
      constexpr int KD = decltype(kind)::value;
      const char* kc_ = smem + cur + l16; const char* kn_ = smem + nxt + l16;
      const char* ac_ = smem + cur + 1024 + lane * 8; const char* an_ = smem + nxt + 1024 + lane * 8;
      const char* vc_ = smem + cur + vrd; const char* vn_ = smem + nxt + vrd;
      auto hook = [&](auto pc) {
        constexpr int p = decltype(pc)::value, u = p >> 4, q = p & 15;
        SB();
        if constexpr (KD < 2) {
          if constexpr (p == 1) {
            if constexpr (KD == 0) asm volatile("s_waitcnt vmcnt(0)" : TIE_KV(sg) :: "memory");
            else asm volatile("s_waitcnt vmcnt(7)" : TIE_KV(sg), TIE_G(0) :: "memory");
          }
          if constexpr (p == 2) wr_k(sg, smem + nxt + w * KVT);
          if constexpr (p == 3) wr_v(sg, smem + nxt + w * KVT);
          if constexpr (p == 5) ld_k(sg, min(s + 2, 7) * 8 + w);
          if constexpr (p == 6) ld_v(sg, min(s + 2, 7) * 8 + w);
          if constexpr (p == 23) BARRIER();
        }
        if constexpr (KD == 0) {
          if constexpr (p == 7) ld8(ffw, ws, fwi);
          if constexpr (p == 15) {
            asm volatile("s_waitcnt vmcnt(0)" : "+v"(ffw) :: "memory");
            const unsigned f0 = ffw[0], f1 = ffw[1];
            if (__builtin_amdgcn_ballot_w64(f0 != tag0 || f1 != tag1) != 0) wait_flags(ws, fwi, tag0, tag1);
          }
          if constexpr (p >= 16 && p < 26) g_ld((p - 16) / 5, (p - 16) % 5);
        } else if constexpr (KD == 1) {
          if constexpr (p == 17) asm volatile("s_waitcnt vmcnt(11)" : TIE_G(1) :: "memory");
          if constexpr (q % 3 == 1) { g_mm(u, q / 3); SB(); g_ld(u, q / 3); }
        } else {
          if constexpr (p == 1) asm volatile("s_waitcnt vmcnt(7)" : TIE_KV(sg), TIE_G(0) :: "memory");
          if constexpr (p == 11) asm volatile("s_waitcnt vmcnt(9)" : TIE_G(1) :: "memory");
          if constexpr (p < 14 && (p & 1)) g_mm(p / 10, p % 10 / 2);
          if constexpr (p == 15) { if (hw == 0) g_mm(1, 2); }
          if constexpr (p >= 2 && p < 27) {
            constexpr int i = p - 2, k = i % 5;
            SB();
            ld16<0>(wb[i], (k == 2 ? po4 : k % 3 == 1 ? po0 + 40 * 1024 : po0) + i / 5 * 2048, k < 3 ? g0 : g1);
          }
        }
        SB();
      };
      sfor<0, 8>([&](auto tc) {
        constexpr int t = decltype(tc)::value;
        constexpr bool NX = KD < 2 || t < 7, NK = KD < 2 || t < 6;
        float e[12];
        f16v sn;
        SB();
        if constexpr (NX) { sn = mfma16(km, qm, f16v{}); PIN(sn); }
        SB();
        if constexpr (NK) km = *(const u4*)(t < 6 ? kc_ + (t + 2) * KVT : kn_ + (t - 6) * KVT);
#pragma unroll
        for (int i = 0; i < 4; ++i) e[i] = __builtin_amdgcn_exp2f(sc[i]);
        hook(std::integral_constant<int, 4 * t>{});
        p1[2] = pk_bf16(e12, e13); p1[3] = pk_bf16(e14, e15);
        SB();
        if constexpr (NX) { sn = mfma8(ka, qa, sn); PIN(sn); }
        SB();
        if constexpr (NK) ka = *(const u2*)(t < 6 ? ac_ + (t + 2) * KVT : an_ + (t - 6) * KVT);
#pragma unroll
        for (int i = 4; i < 8; ++i) e[i] = __builtin_amdgcn_exp2f(sc[i]);
        hook(std::integral_constant<int, 4 * t + 1>{});
        p0[0] = pk_bf16(e[0], e[1]); p0[1] = pk_bf16(e[2], e[3]);
        SB();
        o = mfma16(v1, p1, o);
        SB();
        v1 = rd_frag(vc_ + t * KVT + 256, 128);
#pragma unroll
        for (int i = 8; i < 12; ++i) e[i] = __builtin_amdgcn_exp2f(sc[i]);
        hook(std::integral_constant<int, 4 * t + 2>{});
        p0[2] = pk_bf16(e[4], e[5]); p0[3] = pk_bf16(e[6], e[7]);
        SB();
        o = mfma16(v0, p0, o);
        SB();
        if constexpr (NX) v0 = rd_frag(t < 7 ? vc_ + (t + 1) * KVT : vn_, 128);
        e12 = __builtin_amdgcn_exp2f(sc[12]); e13 = __builtin_amdgcn_exp2f(sc[13]);
        e14 = __builtin_amdgcn_exp2f(sc[14]); e15 = __builtin_amdgcn_exp2f(sc[15]);
        hook(std::integral_constant<int, 4 * t + 3>{});
        p1[0] = pk_bf16(e[8], e[9]); p1[1] = pk_bf16(e[10], e[11]);
        SB();
        if constexpr (NX) sc = sn;
      });
    };
    stage(std::integral_constant<int, 0>{}, 0, L_E, L_E - KVS);
    TS(10);
    {
      int cur = L_E - KVS;
#pragma unroll 1
      for (int s = 1; s < 7; ++s) {
        const int nxt = cur == L_E - 2 * KVS ? L_E : cur - KVS;
        stage(std::integral_constant<int, 1>{}, s, cur, nxt);
        cur = nxt;
      }
    }
    TS(11);
    g0 = l16 + hw * 1024; g1 = l16 + (1 - hw) * 1024;
    stage(std::integral_constant<int, 2>{}, 7, L_E - KVS, 0);
    p1[2] = pk_bf16(e12, e13); p1[3] = pk_bf16(e14, e15);
    o = mfma16(v1, p1, o);
    TS(12); TSC(27);
    {
      float am = 0.f, chk = 0.f;
#pragma unroll
      for (int i = 0; i < 12; ++i) { am = fmaxf(am, fabsf(o[i])); chk += o[i] * 0.f; }
      const bool ok = (o[12] < 1e30f) && (am < 3e38f) && (chk == 0.f);
      if (__builtin_amdgcn_ballot_w64(!ok) != 0) {
        char* tp = smem + w * KVT;
        set_q(u2{0u, 0u});
        float m = -INFINITY;
        KV d;
        d.vw = u4{0u, 0u, 0u, 0u}; d.vh = u2{0u, 0u};
#pragma unroll 1
        for (int t = 0; t < 64; ++t) {
          ld_k(d, t);
          asm volatile("s_waitcnt vmcnt(0)" : TIE_KV(d) :: "memory");
          f16v s0 = mfma16(d.kw, qm, f16v{});
          s0 = mfma8(d.kh, qa, s0);
          m = fmaxf(m, max16(s0));
        }
        m = fmaxf(m, xor32(m));
        set_q(split3(m));
        o = f16v{};
#pragma unroll 1
        for (int t = 0; t < 64; ++t) {
          ld_k(d, t); ld_v(d, t);
          asm volatile("s_waitcnt vmcnt(0)" : TIE_KV(d) :: "memory");
          wr_k(d, tp); wr_v(d, tp);
          asm volatile("" ::: "memory");
          f16v s0 = mfma16(*(const u4*)(tp + l16), qm, f16v{});
          s0 = mfma8(*(const u2*)(tp + 1024 + lane * 8), qa, s0);
#pragma unroll
          for (int e = 0; e < 4; ++e) {
            p0[e] = pk_bf16(__builtin_amdgcn_exp2f(s0[2 * e]), __builtin_amdgcn_exp2f(s0[2 * e + 1]));
            p1[e] = pk_bf16(__builtin_amdgcn_exp2f(s0[8 + 2 * e]), __builtin_amdgcn_exp2f(s0[9 + 2 * e]));
          }
          o = mfma16(rd_frag(tp + vrd, 128), p0, o);
          o = mfma16(rd_frag(tp + vrd + 256, 128), p1, o);
          asm volatile("" ::: "memory");
        }
      }
    }
    {
      const float inv = 1.f / o[12];
      char* ob = ws + OFF_O + qt * 40960;
      st16<0>(ob + head * 1024, l16, u4{pk_bf16(o[0] * inv, o[1] * inv), pk_bf16(o[2] * inv, o[3] * inv),
                                        pk_bf16(o[4] * inv, o[5] * inv), pk_bf16(o[6] * inv, o[7] * inv)});
      if (!hi) st8(ob + (32 + xcd) * 1024 + (hq >> 1) * 512 + (hq & 1) * 8, l16,
                   u2{pk_bf16(o[8] * inv, o[9] * inv), pk_bf16(o[10] * inv, o[11] * inv)});
    }
    WAIT_V(0); BARRIER();
    if (threadIdx.x == 0) st8(ws, OFF_FO + (qb * 32 + head) * 64, u2{tag0, tag1});
    TS(13);
    const int n0 = cg * 160 + hw * 64 + 8 * kg + 4 * hi, n4 = cg * 160 + 128 + 8 * kg + 4 * hi + 2 * hw;
    const size_t ro = (size_t)(rt * 32 + r) * DM;
    const u2 xa = *(const u2*)(x + ro + n0), xb = *(const u2*)(x + ro + n0 + 32);
    const u2 ba = *(const u2*)(b2 + n0), bb = *(const u2*)(b2 + n0 + 32);
    const unsigned xc = *(const unsigned*)(x + ro + n4), bc = *(const unsigned*)(b2 + n4);
    if (b == 0 && w < 4) wait_flags(ws, OFF_FX + (w * 64 + lane) * 64, tag0, tag1);
    wait_flags(ws, OFF_FO + ((rt >> 3) * 32 + r) * 64, tag0, tag1);
    TS(14);
    {
      u4 oa[10];
      const char* pq = ws + OFF_O + (rt * 40 + kg * 10) * 1024;
#pragma unroll
      for (int i = 0; i < 10; ++i) ld16<0>(oa[i], pq + i / 2 * 2048, i & 1 ? g1 : g0);
      sfor<0, 10>([&](auto ic) {
        constexpr int i = decltype(ic)::value, f = i / 2 * 5 + (i & 1) * 3;
        if constexpr (i & 1) asm volatile("s_waitcnt vmcnt(%3)" : "+v"(oa[i]), "+v"(wb[f]), "+v"(wb[f + 1]) : "n"(9 - i) : "memory");
        else asm volatile("s_waitcnt vmcnt(%4)" : "+v"(oa[i]), "+v"(wb[f]), "+v"(wb[f + 1]), "+v"(wb[f + 2]) : "n"(9 - i) : "memory");
        acc[0] = mfma16(wb[f], oa[i], acc[0]);
        acc[1] = mfma16(wb[f + 1], oa[i], acc[1]);
        if constexpr (!(i & 1)) acc[2] = mfma16(wb[f + 2], oa[i], acc[2]);
      });
    }
    TS(15);
#pragma unroll
    for (int q = 0; q < 4; ++q)
#pragma unroll
      for (int j = 0; j < 3; ++j)
        *(f4*)(smem + ((q * 8 + w) * 3 + j) * 1024 + l16) = f4{acc[j][4 * q], acc[j][4 * q + 1], acc[j][4 * q + 2], acc[j][4 * q + 3]};
    WAIT_L0(); BARRIER();
    if (threadIdx.x == 0 && b == 0) st8(ws, OFF_E, u2{tag0, tag1});
    {
      const char* rp = smem + kg * 24576 + l16;
      f4 ra[4], rb[4];
      f2 rc[8];
#pragma unroll
      for (int i = 0; i < 4; ++i) {
        ra[i] = *(const f4*)(rp + hw * 12288 + i * 3072);
        rb[i] = *(const f4*)(rp + hw * 12288 + i * 3072 + 1024);
      }
#pragma unroll
      for (int i = 0; i < 8; ++i) rc[i] = *(const f2*)(rp + hw * 8 + i * 3072 + 2048);
      auto fin = [&](float v0, float v1, unsigned xv, unsigned bv) {
        return pk_bf16(v0 + (bf_lo(xv) + bf_lo(bv)), v1 + (bf_hi(xv) + bf_hi(bv)));
      };
      const f4 va = (ra[0] + ra[1]) + (ra[2] + ra[3]), vb = (rb[0] + rb[1]) + (rb[2] + rb[3]);
      const f2 vc = ((rc[0] + rc[1]) + (rc[2] + rc[3])) + ((rc[4] + rc[5]) + (rc[6] + rc[7]));
      *(u2*)(out + ro + n0) = u2{fin(va[0], va[1], xa[0], ba[0]), fin(va[2], va[3], xa[1], ba[1])};
      *(u2*)(out + ro + n0 + 32) = u2{fin(vb[0], vb[1], xb[0], bb[0]), fin(vb[2], vb[3], xb[1], bb[1])};
      *(unsigned*)(out + ro + n4) = fin(vc[0], vc[1], xc, bc);
    }
    TS(16); TSC(25);
  }
}

extern "C" void kernel_launch(void* const* d_in, const int* in_sizes, int n_in, void* d_out, int out_size,
                              void* d_ws, size_t ws_size, hipStream_t stream) {
  mega_kernel<<<256, 512, 0, stream>>>((const u16*)d_in[0], (const u16*)d_in[1], (const u16*)d_in[2], (const u16*)d_in[4],
                                       (const u16*)d_in[5], (const u16*)d_in[6], (const u16*)d_in[7], (u16*)d_out, (char*)d_ws);
}
```

```cpp
#include <hip/hip_runtime.h>
#include <hip/hip_bf16.h>
#include <stdint.h>
#include <type_traits>

typedef __bf16 bf2 __attribute__((ext_vector_type(2)));
typedef __bf16 bf8 __attribute__((ext_vector_type(8)));
typedef short s4 __attribute__((ext_vector_type(4)));
typedef float f2 __attribute__((ext_vector_type(2)));
typedef float f4 __attribute__((ext_vector_type(4)));
typedef float f16v __attribute__((ext_vector_type(16)));
typedef unsigned u2 __attribute__((ext_vector_type(2)));
typedef unsigned u4 __attribute__((ext_vector_type(4)));
typedef unsigned short u16;
typedef unsigned long long u64;
#define LDS_AS __attribute__((address_space(3)))
#define DEVINL __device__ __forceinline__

constexpr int DM = 640, FF = 1728;
constexpr unsigned OFF_E  = 0;
constexpr unsigned OFF_FX = 4096;
constexpr unsigned OFF_FW = OFF_FX + 16384;
constexpr unsigned OFF_RS = 65536;
constexpr unsigned OFF_CS = OFF_RS + 2048 * 16;
constexpr unsigned OFF_X  = 262144;
constexpr unsigned OFF_H  = OFF_X + 64 * 120 * 1024;
constexpr unsigned OFF_W2 = OFF_H + 64 * 108 * 1024;
constexpr unsigned OFF_WO = OFF_W2 + 20 * 108 * 1024;
constexpr unsigned OFF_O  = OFF_WO + 20 * 40 * 1024;
constexpr unsigned OFF_DUM = OFF_O + 64 * 40 * 1024;

#define WAIT_V(n) asm volatile("s_waitcnt vmcnt(%0)" :: "n"(n) : "memory")
#define WAIT_L0() do { asm volatile("" ::: "memory"); __builtin_amdgcn_s_waitcnt(0xc07f); asm volatile("" ::: "memory"); } while (0)
#define BARRIER() do { asm volatile("" ::: "memory"); __builtin_amdgcn_s_barrier(); asm volatile("" ::: "memory"); } while (0)
#define SB() __builtin_amdgcn_sched_barrier(0)

template <int I, int N, class F> DEVINL void sfor(F&& f) {
  if constexpr (I < N) { f(std::integral_constant<int, I>{}); sfor<I + 1, N>(f); }
}
DEVINL unsigned pk_bf16(float a, float b) {
  f2 f = {a, b};
  bf2 r = __builtin_convertvector(f, bf2);
  return __builtin_bit_cast(unsigned, r);
}
DEVINL float bf_lo(unsigned v) { return __builtin_bit_cast(float, v << 16); }
DEVINL float bf_hi(unsigned v) { return __builtin_bit_cast(float, v & 0xffff0000u); }
DEVINL f16v mfma16(u4 a, u4 b, f16v c) {
  return __builtin_amdgcn_mfma_f32_32x32x16_bf16(__builtin_bit_cast(bf8, a), __builtin_bit_cast(bf8, b), c, 0, 0, 0);
}
DEVINL f16v mfma8(u2 a, u2 b, f16v c) {
  return __builtin_amdgcn_mfma_f32_32x32x8bf16_1k(__builtin_bit_cast(s4, a), __builtin_bit_cast(s4, b), c, 0, 0, 0);
}
DEVINL f4 mfma_s(u4 a, u4 b, f4 c) {
  return __builtin_amdgcn_mfma_f32_16x16x32_bf16(__builtin_bit_cast(bf8, a), __builtin_bit_cast(bf8, b), c, 0, 0, 0);
}
DEVINL f4 mfma_q(u2 a, f4 c) {
  return __builtin_amdgcn_mfma_f32_4x4x4bf16_1k(__builtin_bit_cast(s4, a), __builtin_bit_cast(s4, a), c, 0, 0, 0);
}
DEVINL void mm2(f16v& c0, f16v& c1, u4 a, u4 b0, u4 b1, unsigned skip) {
  asm volatile("s_cmp_lg_u32 %5, 0\n\ts_cbranch_scc1 .Lmm%=\n\tv_mfma_f32_32x32x16_bf16 %0, %2, %3, %0\n\t"
               "v_mfma_f32_32x32x16_bf16 %1, %2, %4, %1\n.Lmm%=:"
               : "+v"(c0), "+v"(c1) : "v"(a), "v"(b0), "v"(b1), "s"(skip) : "scc");
}
DEVINL void mm3(f16v& c0, f16v& c1, f16v& c2, u4 a, u4 b0, u4 b1, u4 b2, unsigned skip) {
  asm volatile("s_cmp_lg_u32 %7, 0\n\ts_cbranch_scc1 .Lmm%=\n\tv_mfma_f32_32x32x16_bf16 %0, %3, %4, %0\n\t"
               "v_mfma_f32_32x32x16_bf16 %1, %3, %5, %1\n\tv_mfma_f32_32x32x16_bf16 %2, %3, %6, %2\n.Lmm%=:"
               : "+v"(c0), "+v"(c1), "+v"(c2) : "v"(a), "v"(b0), "v"(b1), "v"(b2), "s"(skip) : "scc");
}
template <int N> DEVINL void wait_v(u4& a) { asm volatile("s_waitcnt vmcnt(%1)" : "+v"(a) : "n"(N) : "memory"); }
DEVINL float xor32(float v) { return __shfl_xor(v, 32); }
DEVINL unsigned lds_addr(const void* p) { return (unsigned)(size_t)(const LDS_AS char*)p; }
#define NTA 0
#define NTB 0
#define NTR 0
template <int NT> DEVINL void glds16(const char* gbase, unsigned voff, unsigned lds) {
  if constexpr (NT) asm volatile("s_mov_b32 m0, %0\n\ts_nop 0\n\tglobal_load_lds_dwordx4 %1, %2 nt" ::"s"(lds), "v"(voff), "s"(gbase) : "memory");
  else asm volatile("s_mov_b32 m0, %0\n\ts_nop 0\n\tglobal_load_lds_dwordx4 %1, %2" ::"s"(lds), "v"(voff), "s"(gbase) : "memory");
}
DEVINL void glds16m(const char* gbase, unsigned voff, unsigned lds, u64 mask) {
  asm volatile("s_mov_b32 m0, %0\n\ts_mov_b64 exec, %3\n\tglobal_load_lds_dwordx4 %1, %2\n\ts_mov_b64 exec, -1"
               ::"s"(lds), "v"(voff), "s"(gbase), "s"(mask) : "memory");
}
DEVINL void glds4(const char* gbase, unsigned voff, unsigned lds) {
  asm volatile("s_mov_b32 m0, %0\n\ts_nop 0\n\tglobal_load_lds_dword %1, %2" ::"s"(lds), "v"(voff), "s"(gbase) : "memory");
}
DEVINL void ldp(u4& d, const void* g, unsigned v) { asm volatile("global_load_dwordx4 %0, %1, %2" : "=v"(d) : "v"(v), "s"(g) : "memory"); }
DEVINL void ldp(u2& d, const void* g, unsigned v) { asm volatile("global_load_dwordx2 %0, %1, %2" : "=v"(d) : "v"(v), "s"(g) : "memory"); }
DEVINL void ldp(unsigned& d, const void* g, unsigned v) { asm volatile("global_load_dword %0, %1, %2" : "=v"(d) : "v"(v), "s"(g) : "memory"); }
DEVINL void ldp2(unsigned& d, const void* g, unsigned v) { asm volatile("global_load_ushort %0, %1, %2" : "=v"(d) : "v"(v), "s"(g) : "memory"); }
template <int OFF = 0> DEVINL void ld16(u4& d, const char* gbase, unsigned voff) {
  asm volatile("global_load_dwordx4 %0, %1, %2 offset:%3 sc1" : "=v"(d) : "v"(voff), "s"(gbase), "n"(OFF) : "memory");
}
DEVINL void ld8(u2& d, const char* gbase, unsigned voff) {
  asm volatile("global_load_dwordx2 %0, %1, %2 sc1" : "=v"(d) : "v"(voff), "s"(gbase) : "memory");
}
DEVINL void ldif(u4& d, const char* gbase, unsigned voff, unsigned skip) {
  asm volatile("s_cmp_lg_u32 %3, 0\n\ts_cbranch_scc1 .Lcl%=\n\tglobal_load_dwordx4 %0, %1, %2 sc1\n.Lcl%=:"
               : "+v"(d) : "v"(voff), "s"(gbase), "s"(skip) : "scc", "memory");
}
DEVINL void ldif(u2& d, const char* gbase, unsigned voff, unsigned skip) {
  asm volatile("s_cmp_lg_u32 %3, 0\n\ts_cbranch_scc1 .Lcl%=\n\tglobal_load_dwordx2 %0, %1, %2 sc1\n.Lcl%=:"
               : "+v"(d) : "v"(voff), "s"(gbase), "s"(skip) : "scc", "memory");
}
template <int OFF = 0> DEVINL void st16(char* gbase, unsigned voff, u4 v) {
  asm volatile("global_store_dwordx4 %0, %1, %2 offset:%3 sc1\n\ts_nop 1" ::"v"(voff), "v"(v), "s"(gbase), "n"(OFF) : "memory");
}
DEVINL void st8(char* gbase, unsigned voff, u2 v) {
  asm volatile("global_store_dwordx2 %0, %1, %2 sc1\n\ts_nop 1" ::"v"(voff), "v"(v), "s"(gbase) : "memory");
}
template <int OFF> DEVINL void wr_tid(float v) {
  asm volatile("ds_write_addtid_b32 %0 offset:%1" :: "v"(v), "n"(OFF) : "memory");
}
DEVINL u2 tr_read(const char* l) {
  s4 r = __builtin_amdgcn_ds_read_tr16_b64_v4i16((LDS_AS s4*)l);
  return __builtin_bit_cast(u2, r);
}
DEVINL void wait_flags(const char* ws, unsigned voff, unsigned t0, unsigned t1) {
#pragma unroll 1
  for (int spin = 0; spin < (1 << 19); ++spin) {
    u2 f;
    asm volatile("global_load_dwordx2 %0, %1, %2 sc1\n\ts_waitcnt vmcnt(0)" : "=&v"(f) : "v"(voff), "s"(ws) : "memory");
    const unsigned f0 = f[0], f1 = f[1];
    if (__builtin_amdgcn_ballot_w64(f0 != t0 || f1 != t1) == 0) break;
    __builtin_amdgcn_s_sleep(2);
  }
  asm volatile("" ::: "memory");
}
#define TRE 1
#define TRB (TRE ? 16 : 7)
#define TSM (TRE ? 0xffffff : 0xff)
#define TS(i)
#define TSC(i)
#define TR(i)
#define TS0A()
#define TS0B()
#define TRD(n)

constexpr int L_DUM = 131072;
constexpr int L_ST = L_DUM + 256;
constexpr int L_CS = L_ST + 2048;
#define PIN(v) asm volatile("" : "+v"(v))
constexpr int KVT = 3200, KVS = -8 * KVT;
constexpr int L_E = L_CS + 4096;
constexpr int L_TOTAL = L_E - KVS;
#define OSC 1
#define ROT 6
#define XF 11
#define TRM 0
#define GS 5
#define FXP 148
#define FX1 136
#define NB 1
#define FXC 147
#define ESP 1
constexpr int NE = (159 - FXC + ESP - 1) / ESP < 10 ? (159 - FXC + ESP - 1) / ESP : 10;
#define HSP 0
#define RSL 0
#define KILL(v) asm volatile("" : "=v"(v))
struct KV { u4 kw, vw; u2 kh, vh; };
#define FWP 15
#define FWN (FWP > 18 ? 31 - FWP : 13)
#define MPOS 2, 4, 8, 10, 12, 14, 16, 18, 20, 24, 26, 28, 30
constexpr int mslot(int p) {
  constexpr int pos[13] = {MPOS};
  for (int i = 0; i < 13; ++i) if (pos[i] == p) return i;
  return -1;
}
#define XLP 24
#define PSL 0
#define PK 10
#define PS2 2
#define B1 1
#define HFP 12
#define PPA 10, 18, 22
#define PPB 12, 22, 28
#define PPC 2, 14, 19
#define PPE 1
constexpr int prio_at(int kd, int p) {
  constexpr int a[3] = {PPA}, l[3] = {PPB}, c[3] = {PPC}, z[3] = {6, 9, 11};
  for (int i = 0; i < 3; ++i)
    if (p == (kd == 0 ? z[i] : kd == 2 ? l[i] : kd == 3 ? c[i] : a[i])) return 2 - i;
  return -1;
}
#define ORT 1
#define HK 2
#define CV 2
#define W0 3
#define DB 1
#define RPK 1
#define CWLIM 30960

DEVINL u4 rd_frag(const char* p, int second) {
  const u2 lo = tr_read(p), hh = tr_read(p + second);
  return u4{lo[0], lo[1], hh[0], hh[1]};
}
DEVINL unsigned split2(float c) {
  const float c1 = bf_lo(pk_bf16(c, c));
  return pk_bf16(c1, c - c1);
}
DEVINL u2 split3(float m) {
  const float a = -m;
  const unsigned h1 = __builtin_bit_cast(unsigned, a) & 0xffff0000u;
  const float r1 = a - __builtin_bit_cast(float, h1);
  const unsigned h2 = __builtin_bit_cast(unsigned, r1) & 0xffff0000u;
  const float r2 = r1 - __builtin_bit_cast(float, h2);
  const unsigned h3 = __builtin_bit_cast(unsigned, r2) & 0xffff0000u;
  return u2{(h1 >> 16) | h2, h3 >> 16};
}
DEVINL float max16(const f16v& s) {
  float m = s[0];
#pragma unroll
  for (int i = 1; i < 16; ++i) m = fmaxf(m, s[i]);
  return m;
}
DEVINL float gelu_a(float a, float k1, float k3) { return __builtin_amdgcn_exp2f(a * (a * a * k3 + k1)); }
DEVINL float gelu_b(float a, float e, float sig) { return a * __builtin_amdgcn_rcpf(e * sig + sig); }

DEVINL void swap32(unsigned& a, unsigned& b) {
  const auto r = __builtin_amdgcn_permlane32_swap(a, b, false, false);
  a = r[0]; b = r[1];
}
DEVINL void swap32(float& a, float& b) {
  unsigned x = __builtin_bit_cast(unsigned, a), y = __builtin_bit_cast(unsigned, b);
  swap32(x, y);
  a = __builtin_bit_cast(float, x); b = __builtin_bit_cast(float, y);
}
DEVINL void swap16(float& a, float& b) {
  const auto r = __builtin_amdgcn_permlane16_swap(__builtin_bit_cast(unsigned, a), __builtin_bit_cast(unsigned, b), false, false);
  const unsigned x = r[0], y = r[1];
  a = __builtin_bit_cast(float, x); b = __builtin_bit_cast(float, y);
}
template <int C> DEVINL float dpp(float v) {
  return __builtin_bit_cast(float, __builtin_amdgcn_update_dpp(0, __builtin_bit_cast(int, v), C, 0xf, 0xf, true));
}
DEVINL void gelu_tile(const f16v& a, float rstd, float sig, u4& o0, u4& o1) {
  const float k1 = rstd * -2.3022081986f, k3 = rstd * rstd * rstd * -0.1029432397f;
  f2 v[8], t[8];
#pragma unroll
  for (int e = 0; e < 8; ++e) { v[e] = f2{a[2 * e], a[2 * e + 1]}; t[e] = v[e] * v[e]; }
  SB();
#pragma unroll
  for (int e = 0; e < 8; ++e) t[e] = __builtin_elementwise_fma(t[e], f2{k3, k3}, f2{k1, k1});
  SB();
#pragma unroll
  for (int e = 0; e < 8; ++e) t[e] *= v[e];
  SB();
#pragma unroll
  for (int e = 0; e < 8; ++e) t[e] = f2{__builtin_amdgcn_exp2f(t[e][0]), __builtin_amdgcn_exp2f(t[e][1])};
  SB();
#pragma unroll
  for (int e = 0; e < 8; ++e) t[e] = __builtin_elementwise_fma(t[e], f2{sig, sig}, f2{sig, sig});
  SB();
#pragma unroll
  for (int e = 0; e < 8; ++e) t[e] = f2{__builtin_amdgcn_rcpf(t[e][0]), __builtin_amdgcn_rcpf(t[e][1])};
  SB();
#pragma unroll
  for (int e = 0; e < 8; ++e) t[e] *= v[e];
  SB();
#pragma unroll
  for (int e = 0; e < 4; ++e) { o0[e] = pk_bf16(t[e][0], t[e][1]); o1[e] = pk_bf16(t[4 + e][0], t[4 + e][1]); }
}

__global__ __launch_bounds__(512, 2) void mega_kernel(const u16* __restrict__ x, const u16* __restrict__ wqkv,
                                                     const u16* __restrict__ wo, const u16* __restrict__ w1,
                                                     const u16* __restrict__ b1, const u16* __restrict__ w2,
                                                     const u16* __restrict__ b2, u16* __restrict__ out,
                                                     char* __restrict__ ws) {
  __shared__ __attribute__((aligned(16))) char smem[L_TOTAL];
  int lane = threadIdx.x & 63;
  int w = __builtin_amdgcn_readfirstlane(threadIdx.x >> 6);
  int b = blockIdx.x;
  TS0A();
  SB();
  asm volatile("" : "+s"(b));
  SB();
  int xcd = b & 7, idx = b >> 3;
  const unsigned lb = lds_addr(smem);
  unsigned tag0, tag1;
  unsigned tr_[16];
  int rb = (xcd & 1) * 8 + (idx & 7), g = (xcd >> 1) + 4 * (idx >> 3);
  int hq, head, qt, fw, fh;
  f16v acc1[2];
  float rstd, sig;
  u2 fx;
  unsigned fxi;
  char* hp;
  u4 qm; u2 qa;
  KV sg, sg0;
  unsigned vw, vh, late = 1;
  bool dummy;
  auto early = [&](auto ic, unsigned skip) {
    constexpr int i = decltype(ic)::value;
    const char* p = ws + OFF_X + (i == 4 || i == 5 ? qt * 120 : (w + (i > 5 ? 8 : 0)) * 120 + ((i + (i > 5 ? 2 : 0)) & 2 ? 80 : 40)) * 1024;
    if constexpr (i == 0) ldif(sg0.kw, p, vw, skip);
    if constexpr (i == 1) ldif(sg0.kh, p, vh, skip);
    if constexpr (i == 2) ldif(sg0.vw, p, vw, skip);
    if constexpr (i == 3) ldif(sg0.vh, p, vh, skip);
    if constexpr (i == 4) ldif(qm, p, vw, skip);
    if constexpr (i == 5) ldif(qa, p, vh, skip);
    if constexpr (i == 6) ldif(sg.kw, p, vw, skip);
    if constexpr (i == 7) ldif(sg.kh, p, vh, skip);
    if constexpr (i == 8) ldif(sg.vw, p, vw, skip);
    if constexpr (i == 9) ldif(sg.vh, p, vh, skip);
  };
  auto early0 = [&]() {
    KILL(sg0.kw); KILL(sg0.kh); KILL(sg0.vw); KILL(sg0.vh); KILL(qm); KILL(qa); KILL(sg.kw); KILL(sg.kh); KILL(sg.vw); KILL(sg.vh);
    PIN(lane);
    vw = lane * 16 + fw * 1024; vh = lane * 16 + fh * 1024 + (hq >> 1) * 8;
    PIN(vw); PIN(vh);
  };
  {
    int un2 = 30 + min(2 * g, g + 9) + max(g - 13, 0);
    int un0 = g < 10 ? 10 + g : min(2 * g - 20, g - 6);
    int un1 = g < 14 ? un0 + (g < 10 ? 10 : 1) : un2 - 1;
    int brow = 8 * w + (lane >> 3);
    unsigned bchunk = ((lane & 7) ^ (((brow >> 1) & 1) << 2)) * 16;
    unsigned l16 = lane * 16;
    int k0 = ((idx + (idx >> 3)) & 1);
    auto kc_of = [&](int s) { const int k = k0 + s; return k >= 10 ? k - 10 : k; };
    int ld1 = g < 14 ? DM * 6 : FF * 2;
    unsigned va[2];
#pragma unroll
    for (int i = 0; i < 2; ++i) {
      const int row = 16 * w + 8 * i + (lane >> 3);
      va[i] = (rb * 128 + row) * (DM * 2) + 16 * ((lane & 7) ^ ((row >> 1) & 7));
    }
    unsigned vx = brow * (DM * 6) + bchunk + un0 * 128, v1 = brow * ld1 + bchunk + (g < 14 ? un1 : un1 - 30) * 128;
    int srow = (xcd & 1) * 1024 + ((xcd >> 1) * 32 + idx) * 8 + w;
    int cu = ((un0 | un1 << 8 | un2 << 16 | (un2 + (g < 9)) << 24) >> ((idx & 6) * 4)) & 255;
    const int cq = (xcd & 1) * 2 + (idx & 1);
    const int cld = cu < 30 ? DM * 6 : FF * 2;
    unsigned cv[5], lx = srow * (DM * 2) + l16, l4 = srow * (DM * 2) + lane * 4 + 1024, sdst, vz = OFF_E;
    cv[0] = (cq * 160 + w * 20 + (lane >> 4)) * cld + (lane & 15) * 8 + (cu < 30 ? cu : cu - 30) * 128;
#pragma unroll
    for (int i = 1; i < 5; ++i) cv[i] = cv[i - 1] + 4 * cld;
    unsigned ko[3] = {k0 * 128u, k0 * (64u * DM * 6), k0 * 64u * ld1}, lo[2] = {lb + w * 2048, lb + w * 1024 + 16384};
    unsigned xr1;
    u4 xr0;
    u2 wr[5], ev;
    if constexpr (TRM == 2) { SB(); TR(3); SB(); }
    asm volatile(
        "s_mov_b32 m0, %[l0]\n\ts_cmp_lt_u32 %[g], 14\n\tglobal_load_lds_dwordx4 %[a0], %[x]\n\t"
        "s_cselect_b64 vcc, %[q], %[f]\n\ts_add_u32 m0, m0, 0x400\n\ts_nop 0\n\tglobal_load_lds_dwordx4 %[a1], %[x]\n\t"
        "s_mov_b32 m0, %[l1]\n\ts_nop 0\n\tglobal_load_lds_dwordx4 %[b0], %[q]\n\t"
        "s_add_u32 m0, m0, 0x2000\n\ts_cmp_lt_u32 %[cu], 30\n\tglobal_load_lds_dwordx4 %[b1], vcc\n\t"
        "s_cselect_b64 vcc, %[q], %[f]\n\t"
        "global_load_dwordx2 %0, %[c0], vcc\n\tglobal_load_dwordx2 %1, %[c1], vcc\n\tglobal_load_dwordx2 %2, %[c2], vcc\n\t"
        "global_load_dwordx2 %3, %[c3], vcc\n\tglobal_load_dwordx2 %4, %[c4], vcc\n\t"
        "global_load_dwordx4 %5, %[lx], %[x]\n\tglobal_load_dword %6, %[l4], %[x]\n\tglobal_load_dwordx2 %7, %[vz], %[ws] sc1"
        : "=&v"(wr[0]), "=&v"(wr[1]), "=&v"(wr[2]), "=&v"(wr[3]), "=&v"(wr[4]), "=&v"(xr0), "=&v"(xr1), "=&v"(ev)
        : [x] "s"(x), [q] "s"(wqkv), [f] "s"(w1), [ws] "s"(ws), [cu] "s"(cu), [g] "s"(g), [l0] "s"(lo[0]), [l1] "s"(lo[1]),
          [a0] "v"(va[0] + ko[0]), [a1] "v"(va[1] + ko[0]), [b0] "v"(vx + ko[1]), [b1] "v"(v1 + ko[2]),
          [c0] "v"(cv[0]), [c1] "v"(cv[1]), [c2] "v"(cv[2]), [c3] "v"(cv[3]),
          [c4] "v"(cv[4]), [lx] "v"(lx), [l4] "v"(l4), [vz] "v"(vz) : "vcc", "scc", "memory");
    if constexpr (TRM == 2) { SB(); TR(7); SB(); }
    asm volatile("" : "+s"(x), "+s"(wqkv), "+s"(wo), "+s"(w1), "+s"(b1), "+s"(w2), "+s"(b2), "+s"(out), "+s"(ws),
                 "+s"(w), "+s"(b), "+s"(rb), "+s"(g), "+s"(un0), "+s"(un1), "+s"(un2), "+s"(cu), "+s"(srow), "+s"(k0), "+s"(ld1));
    asm volatile("" : "+v"(lane), "+v"(l16), "+v"(brow), "+v"(bchunk));
    xcd = b & 7; idx = b >> 3;
    hq = idx & 3; head = xcd * 4 + hq; qt = (idx >> 2) * 8 + w;
    fw = xcd * 5 + hq + (hq >> 1); fh = xcd * 5 + ((hq + 3) >> 1);
    const int rq = w & 3, ch = w >> 2, r = lane & 31, hi = lane >> 5;
    const bool four = g < 9, spare = !four && ch;
    const unsigned vw1 = brow * (FF * 2) + bchunk + (un2 - 30) * 128;
    const char* xg = (const char*)x;
    const char* sb0 = (const char*)wqkv;
    const char* sb1 = (const char*)(g < 14 ? wqkv : w1);
    const char* sb2 = (const char*)w1;
    const u64 m3 = four ? ~0ull : 1ull;
    const char* sb3 = sb2 + (four ? 128 : 0);
    const unsigned l3 = four ? lb + 8192 + w * 1024 : lb + L_DUM, s3 = four ? 16384 : 0;
    auto dma = [&](auto sc_, auto ic_) {
      constexpr int s = decltype(sc_)::value, i = decltype(ic_)::value, c = s & 3;
      const unsigned db = lb + c * 32768 + w * 1024;
      if constexpr (s < 10) {
        const int kc = kc_of(s);
        if constexpr (i < 2) glds16<NTA>(xg + kc * 128, va[i], db + (w + i) * 1024);
        if constexpr (i == 2) glds16<NTB>(sb0 + kc * (64 * DM * 6), vx, db + 16384);
        if constexpr (i == 3) glds16<NTB>(sb1 + kc * 64 * ld1, v1, db + 24576);
      } else {
        const int kc = kc_of(2 * (s - 10) + (i >> 1));
        if constexpr (!(i & 1)) glds16<NTB>(sb2 + kc * (64 * FF * 2), vw1, db + (i >> 1) * 16384);
        else glds16m(sb3 + kc * (64 * FF * 2), vw1, l3 + (2 * c + (i >> 1)) * s3, m3);
      }
    };
    auto dma4 = [&](auto sc_) { sfor<0, 4>([&](auto i) { dma(sc_, i); }); };
    u2 pub;
    float sa[4];
    if constexpr (NB == 3) dma4(std::integral_constant<int, 1>{});
    SB();
    {
      const unsigned d0 = OFF_RS + srow * 16 + (lane >> 5) * 8, d1 = OFF_CS + (cu * 4 + (xcd & 1) * 2 + (idx & 1)) * 512 + (w * 8 + (lane >> 3)) * 8;
      sdst = d1 + (lane & 1) * (d0 - d1);
    }
    int lw = L_ST + ((lane & 15) * 4 + (lane >> 4)) * 32 + w * 4, lr = L_ST + w * 256 + lane * 4;
    unsigned cwv = min(((idx * 8 + w) * 2 + (lane & 1)) * 72, CWLIM);
    PIN(sdst); PIN(lw); PIN(lr); PIN(cwv);
    auto stat = [&](auto kc_) {
      constexpr int k = decltype(kc_)::value;
      if constexpr (k == 0) {
#pragma unroll
        for (int i = 0; i < 10; ++i) {
          const unsigned v = wr[i >> 1][i & 1];
          const int e = (i & 1) * 2;
          sa[e] = i < 2 ? bf_lo(v) : sa[e] + bf_lo(v); sa[e + 1] = i < 2 ? bf_hi(v) : sa[e + 1] + bf_hi(v);
        }
        swap32(sa[0], sa[2]); swap32(sa[1], sa[3]);
        sa[0] += sa[2]; sa[1] += sa[3];
        swap16(sa[0], sa[1]);
        *(float*)(smem + lw) = sa[0] + sa[1];
#pragma unroll
        for (int i = 0; i < 5; ++i) {
          const unsigned v = i < 4 ? xr0[i & 3] : xr1;
          const float lo = bf_lo(v), hh = bf_hi(v);
          sa[0] = i == 0 ? lo + hh : sa[0] + (lo + hh);
          sa[1] = __builtin_fmaf(hh, hh, i == 0 ? lo * lo : __builtin_fmaf(lo, lo, sa[1]));
        }
        swap32(sa[0], sa[1]);
        sa[0] += sa[1]; sa[1] = sa[0];
        PIN(sa[1]);
        swap16(sa[0], sa[1]);
        sa[0] += sa[1];
        sa[0] += dpp<0x128>(sa[0]);
        PIN(sa[0]);
        const unsigned e0 = __builtin_amdgcn_readfirstlane(ev[0]), e1 = __builtin_amdgcn_readfirstlane(ev[1]);
        const u64 tg = (((u64)e1 << 32) | e0) + 0x9E3779B97F4A7C15ull;
        tag0 = (unsigned)tg; tag1 = (unsigned)(tg >> 32);
      }
      if constexpr (k == 4) { sa[3] = *(const float*)(smem + lr); sa[0] += dpp<0x124>(sa[0]); PIN(sa[0]); }
      if constexpr (k == 5) { sa[0] += dpp<0x122>(sa[0]); sa[3] += dpp<0xb1>(sa[3]); PIN(sa[0]); PIN(sa[3]); }
      if constexpr (k == 6) { sa[0] += dpp<0x121>(sa[0]); sa[3] += dpp<0x4e>(sa[3]); PIN(sa[0]); PIN(sa[3]); }
      if constexpr (k == 7) {
        sa[3] += dpp<0x141>(sa[3]);
        pub = u2{__builtin_bit_cast(unsigned, lane & 1 ? sa[0] : sa[3]), tag0};
        st8(ws, sdst, pub);
      }
      if constexpr (k == 14) asm volatile("" :: "v"(sdst), "v"(pub));
    };
    auto bsw = [&]() {
      const int blk = (lane >> 4) & 1, q = (lane & 15) >> 2, p = lane & 3;
      return (8 * (lane >> 5) + q) * 128 + ((blk * 2 + (p >> 1)) ^ (((q >> 1) & 1) << 2)) * 16 + (p & 1) * 8;
    };
    int baddr[2], aaddr[4];
    baddr[0] = ch * 8192 + bsw();
    baddr[1] = baddr[0] ^ 64;
    PIN(baddr[0]); PIN(baddr[1]);
#pragma unroll
    for (int t = 0; t < 4; ++t) {
      aaddr[t] = (rq * 32 + r) * 128 + 16 * ((2 * t + hi) ^ ((r >> 1) & 7));
      PIN(aaddr[t]);
    }
    const int rt = rb * 4 + rq, ua = ch ? un1 : un0, ub = un2 + (ch & four);
    u4 fa[40], fb[80][2];
    u4 rs, xo[4];
    u2 cs[2];
    unsigned bias[2];
    u2 auga[2], augb;
    auto rd = [&](auto mc_) {
      constexpr int m = decltype(mc_)::value, q = m < 40 ? m >> 2 : 5 + (m >> 3), so = (q & 1) * 32768;
      if constexpr ((m & 7) == 0 && m > 0 && !(q & 1)) {
        baddr[0] ^= 65536; baddr[1] ^= 65536;
        PIN(baddr[0]); PIN(baddr[1]);
        if constexpr (m < 40) {
#pragma unroll
          for (int t = 0; t < 4; ++t) { aaddr[t] ^= 65536; PIN(aaddr[t]); }
        }
      }
      constexpr int bo = so + (m < 40 ? 1 : (m >> 2) & 1) * 16384 + (m & 3) * 2048;
      fb[m][0] = rd_frag(smem + bo + baddr[0], 512);
      if constexpr (m < 40) fa[m] = *(const u4*)(smem + so + aaddr[m & 3]);
      fb[m][1] = rd_frag(smem + bo + baddr[1], 512);
    };
    auto ld_st = [&]() {
      ld16<0>(rs, ws + OFF_RS + rt * 512, (lane & 31) * 16);
      ld8(cs[0], ws + OFF_CS + (ua * 4 + rq) * 512, lane * 8);
      ld8(cs[1], ws + OFF_CS + (ub * 4 + rq) * 512, lane * 8);
    };
    auto stale = [&]() {
      const unsigned t0 = rs[1], t1 = rs[3], t2 = cs[0][1], t3 = cs[1][1];
      return __builtin_amdgcn_ballot_w64((((t0 ^ tag0) | (t1 ^ tag0)) | ((t2 ^ tag0) | (t3 ^ tag0))) != 0) != 0;
    };
    f4 c4;
    auto rd_cs = [&](int ps) { c4 = *(const f4*)(smem + L_CS + ps * 2048 + ch * 1024 + l16); };
    auto mk_auga = [&](unsigned bz) {
      const unsigned c12 = split2((c4[0] + c4[1]) + (c4[2] + c4[3]));
      unsigned a0 = c12, a1 = c12, b0 = bz | (bz << 16), b1_ = bz;
      PIN(a1);
      swap32(a0, b0); swap32(a1, b1_);
      auga[0] = u2{a0, a1}; auga[1] = u2{b0, b1_};
      PIN(auga[0]); PIN(auga[1]);
    };
    float xk;
    auto x_cvt = [&](int i) {
#pragma unroll
      for (int e = 0; e < 4; ++e) xo[i][e] = pk_bf16(acc1[i >> 1][(i & 1) * 8 + 2 * e] * xk, acc1[i >> 1][(i & 1) * 8 + 2 * e + 1] * xk);
      PIN(xo[i]);
    };
    char* xdp = ws + OFF_X + (rt * 120 + ua * 4) * 1024;
    dummy = spare;
    hp = ws + OFF_H + (rt * 108 + (max(spare ? ua : ub, 30) - 30) * 4) * 1024;
    asm volatile("" : "+s"(xdp), "+s"(hp));
    auto ld_fx = [&]() {
      PIN(lane);
      const unsigned u0 = fw >> 2, u1 = fh >> 2, q0 = max(10 + (u0 >> 1), 6 + u0), q1 = max(10 + (u1 >> 1), 6 + u1);
      const unsigned tb = u0 * 0x11u | u1 * 0x1100u | q0 * 0x110000u | q1 * 0x11000000u;
      const int fi = (tb >> (lane >> 4) * 8 & 15) * 16 + (lane < 32 ? lane & 15 : qt >> 2);
      fxi = OFF_FX + fi * 64;
      PIN(fxi);
      ld8(fx, ws, fxi);
    };
    TR(1);
    WAIT_V(NB == 2 ? 0 : NB == 3 ? 12 : 8);
    TR(2);
    BARRIER();
    TS(1);
    if constexpr (TRM == 2) { SB(); TR(8); SB(); }
    rd(std::integral_constant<int, 0>{}); rd(std::integral_constant<int, 1>{});
    sfor<0, 77>([&](auto nc) {
      constexpr int N = decltype(nc)::value, n = N >> 1, j = N & 1, s = N >> 3;
      SB();
      acc1[j] = mfma16(fb[n][j], fa[n], n == 0 ? f16v{} : acc1[j]);
      SB();
      if constexpr (N == 4) {
        if constexpr (TRM == 2) { TR(9); SB(); }
        asm volatile("s_waitcnt vmcnt(5)" : "+v"(ev), "+v"(xr0), "+v"(xr1), "+v"(wr[0]), "+v"(wr[1]), "+v"(wr[2]), "+v"(wr[3]), "+v"(wr[4])
                     : "v"(cv[0]), "v"(cv[1]), "v"(cv[2]), "v"(cv[3]), "v"(cv[4]), "v"(lx), "v"(l4), "v"(vz) : "memory");
        SB();
        if constexpr (TRM == 2) { TR(10); SB(); }
        dma4(std::integral_constant<int, 2>{});
        SB();
        if constexpr (TRM == 2) { TR(11); SB(); }
        stat(std::integral_constant<int, 0>{});
        SB();
        if constexpr (TRM == 2) { TR(12); SB(); }
      }
      if constexpr (N >= 8 && (N & 7) == 4) dma(std::integral_constant<int, s + 3>{}, std::integral_constant<int, 3>{});
      if constexpr (N == 8 * GS + 4) { ld_st(); ldp2(bias[0], b1 + max(ua - 30, 0) * 64, lane * 2); ldp2(bias[1], b1 + (ub - 30) * 64, lane * 2); }
      if constexpr ((N & 7) == 4) {
        if constexpr (s == GS + 2) asm volatile("s_waitcnt vmcnt(8)" : "+v"(rs), "+v"(cs[0]), "+v"(cs[1]), "+v"(bias[0]), "+v"(bias[1]) :: "memory");
        else WAIT_V(s == 0 ? 5 : s == 1 ? 9 : s == GS || s == GS + 1 ? 13 : 8);
        WAIT_L0(); BARRIER();
        if constexpr (TRM == 0 && (s & 1)) TR(3 + (s >> 1));
        if constexpr (TRM == 1) TR(3 + s);
        if constexpr (TRM == 2 && s == 0) TR(13);
      }
      if constexpr (N == 4) stat(nc);
      if constexpr (N == 8 * GS + 28) rd_cs(0);
      if constexpr (j == 0) rd(std::integral_constant<int, n + 2>{});
      if constexpr (N < 4 && NB != 3) dma(std::integral_constant<int, 1>{}, nc);
      if constexpr (N == 3) {
        const char* pc;
        asm volatile("s_getpc_b64 %0" : "=s"(pc));
        glds4(pc, cwv, lb + L_DUM);
      }
      if constexpr (N > 4 && N <= 14) stat(nc);
      if constexpr (N == 7 || N == 8) {
        dma(std::integral_constant<int, 3>{}, std::integral_constant<int, 2 * (N - 7)>{});
        dma(std::integral_constant<int, 3>{}, std::integral_constant<int, 2 * (N - 7) + 1>{});
      }
      if constexpr (N >= 8 && (N & 7) >= 1 && (N & 7) < 4) dma(std::integral_constant<int, s + 3>{}, std::integral_constant<int, (N & 7) - 1>{});
      if constexpr (N == 8 * GS + 21) {
        if (__builtin_expect(stale(), 0)) {
#pragma unroll 1
          for (int spin = 0; spin < (1 << 19); ++spin) {
            __builtin_amdgcn_s_sleep(4);
            ld_st();
            asm volatile("s_waitcnt vmcnt(0)" : "+v"(rs), "+v"(cs[0]), "+v"(cs[1]) :: "memory");
            if (!stale()) break;
          }
        }
        char* p = smem + L_CS + ch * 1024 + rq * 4 + l16;
        *(unsigned*)p = cs[0][0]; *(unsigned*)(p + 2048) = cs[1][0];
      }
      if constexpr (N == 8 * GS + 23) {
        const unsigned r0 = rs[0], r2 = rs[2];
        const float mu = __builtin_bit_cast(float, r0) * (1.f / DM);
        const float ve = fmaxf(__builtin_bit_cast(float, r2) * (1.f / DM) - mu * mu, 0.f) + 1e-5f;
        rstd = __builtin_amdgcn_rsqf(ve);
        sig = ve * rstd;
        xk = mu;
        PIN(xk); PIN(rstd); PIN(sig);
      }
      if constexpr (N == 8 * GS + 24) {
        const unsigned m12 = split2(-xk);
        augb = u2{(m12 & 0xffffu) | (m12 << 16), (m12 >> 16) | (m12 & 0xffff0000u)};
        xk = rstd * (g >= 10 ? 0.22360679774997896f * 1.4426950408889634f : 1.f);
        PIN(augb); PIN(xk);
      }
      if constexpr (N == 8 * GS + 26) {
        const unsigned s12 = split2(sig);
        const float s3_ = sig - bf_lo(s12) - bf_hi(s12);
        const unsigned a0 = augb[0], a1 = augb[1];
        augb = u2{hi ? s12 : a0, hi ? pk_bf16(s3_, 0.f) : a1};
        PIN(augb);
      }
      if constexpr (N == 8 * GS + 31) mk_auga(ua >= 30 ? bias[0] : 0u);
      SB();
    });
    acc1[0] = mfma16(fb[39][0], fa[39], acc1[0]);
    SB();
    rd(std::integral_constant<int, 41>{});
    if constexpr (GS == 6) mk_auga(ua >= 30 ? bias[0] : 0u);
    SB();
    acc1[0] = mfma8(auga[0], augb, acc1[0]);
    acc1[1] = mfma16(fb[38][1], fa[38], acc1[1]);
    SB();
    acc1[1] = mfma16(fb[39][1], fa[39], acc1[1]);
    SB();
    x_cvt(0);
    SB();
    acc1[1] = mfma8(auga[1], augb, acc1[1]);
    SB();
    x_cvt(1);
    SB();
    if constexpr (TRM < 2) TR(TRM ? 13 : 8);
    TS(2);
    if (spare) {
      if (ua < 30) {
        st16<0>(xdp, l16, xo[0]); st16<1024>(xdp, l16, xo[1]);
        x_cvt(2); x_cvt(3);
        st16<2048>(xdp, l16, xo[2]); st16<3072>(xdp, l16, xo[3]);
      }
      PIN(lane);
      const int rj = min((g - 9) * 16 + rb, 109), ru = rj / 11, sl = min(rj - ru * 11 + 11 * rq, 36);
      const int area = 65536 + 8192 + rq * 16384;
      const bool o = sl >= 27;
      const char* pr = (o ? (const char*)wo : (const char*)w2 + sl * (64 * DM * 2)) + ru * 128;
      const unsigned bc = ((lane & 7) ^ (((lane >> 4) & 1) << 2)) * 16;
#pragma unroll
      for (int j = 0; j < 8; ++j) {
        const int br = j * 8 + (lane >> 3), prow = (br & ~12) | ((br & 4) << 1) | ((br & 8) >> 1);
        const int kap = (sl - 27) * 64 + prow, F = kap >> 4, cc = kap & 15;
        const int row0 = 20 * F + cc, row1 = 80 * (F - 32) + 40 * ((cc >> 2) & 1) + 20 * (cc >> 3) + 16 + (cc & 3);
        const int row = F < 32 ? row0 : row1;
        glds16<NTR>(pr, (o ? row : prow) * (DM * 2) + bc, lb + area + j * 1024);
      }
      if (ua < 30) WAIT_V(16); else WAIT_V(12);
      BARRIER();
      dma(std::integral_constant<int, 13>{}, std::integral_constant<int, 0>{});
      dma(std::integral_constant<int, 13>{}, std::integral_constant<int, 2>{});
      if (ua >= 30) {
        gelu_tile(acc1[0], rstd, sig, xo[0], xo[1]);
        gelu_tile(acc1[1], rstd, sig, xo[2], xo[3]);
        st16<0>(hp, l16, xo[0]); st16<1024>(hp, l16, xo[1]); st16<2048>(hp, l16, xo[2]); st16<3072>(hp, l16, xo[3]);
        WAIT_V(14);
      } else WAIT_V(10);
      BARRIER();
      dma(std::integral_constant<int, 14>{}, std::integral_constant<int, 0>{});
      dma(std::integral_constant<int, 14>{}, std::integral_constant<int, 2>{});
      if constexpr (RSL) { if (ua < 30) WAIT_V(4); else WAIT_V(8); }
      else { WAIT_V(2); BARRIER(); }
      const int ra = area + bsw();
      sfor<0, 2>([&](auto hc) {
        constexpr int hf = decltype(hc)::value;
        const int ct = ru * 2 + hf;
        char* dp = ws + (o ? OFF_WO + (ct * 40 + (sl - 27) * 4) * 1024 : OFF_W2 + (ct * 108 + sl * 4) * 1024);
        u4 f[4];
#pragma unroll
        for (int t = 0; t < 4; ++t) f[t] = rd_frag(smem + t * 2048 + (ra ^ (hf * 64)), 512);
        const unsigned lv = lane * 16;
        st16<0>(dp, lv, f[0]); st16<1024>(dp, lv, f[1]); st16<2048>(dp, lv, f[2]); st16<3072>(dp, lv, f[3]);
      });
      if constexpr (RSL) { WAIT_V(10); BARRIER(); }
      WAIT_V(8); BARRIER();
      ld_fx();
      asm volatile("s_waitcnt vmcnt(0)" : "+v"(fx) :: "memory");
#pragma unroll
      for (int i = 0; i < 40; ++i) KILL(fa[i]);
      KILL(fb[40][0]); KILL(fb[40][1]); KILL(fb[41][0]); KILL(fb[41][1]); KILL(xo[0]); KILL(xo[1]);
    }
    int full = (9 - g) * ch;
    asm volatile("" : "+s"(full));
    full = full > 0 ? 0 : ch;
    if (full == 0) {
      sfor<80, 160>([&](auto nc) {
        constexpr int N = decltype(nc)::value, M = N == 81 ? 82 : N == 82 ? 81 : N, n = M >> 1, j = M & 1, s = 5 + (N >> 4);
        SB();
        acc1[j] = mfma16(fb[n][j], fa[n - 40], n == 40 ? f16v{} : acc1[j]);
        SB();
        if constexpr ((N & 15) == 12 && s < 14) {
          if constexpr (s == 13) WAIT_V(FX1 < 140);
          else WAIT_V(s == 10 ? (XF == 10 ? 0 : 8) : s == 11 ? (XF == 12 ? 8 : 4) : (XF == 11 ? 5 : 4));
          WAIT_L0(); BARRIER();
          if constexpr (s == XF) st8(ws, OFF_FX + (g * 16 + rb) * 64, u2{tag0, tag1});
          if constexpr (TRM == 0) TR(s);
        }
        if constexpr (!(N & 1) && (N >> 1) + 2 < 80) rd(std::integral_constant<int, (N >> 1) + 2>{});
        if constexpr (N == 80) { st16<0>(xdp, l16, xo[0]); x_cvt(2); }
        if constexpr (N == 81) { st16<1024>(xdp, l16, xo[1]); x_cvt(3); }
        if constexpr (N == 83) st16<2048>(xdp, l16, xo[2]);
        if constexpr (N == 85) st16<3072>(xdp, l16, xo[3]);
        if constexpr (N == 83 && TRM == 0) TR(9);
        if constexpr (N >= 93 && N < 101 && (N & 1)) dma(std::integral_constant<int, 13>{}, std::integral_constant<int, (N - 93) / 2>{});
        if constexpr (N >= 109 && N < 117 && (N & 1)) dma(std::integral_constant<int, 14>{}, std::integral_constant<int, (N - 109) / 2>{});
        if constexpr (N == 127) rd_cs(1);
        if constexpr (N == 131) mk_auga(bias[1]);
        if constexpr (N == FX1) ld_fx();
        if constexpr (N == FXC - 1) early0();
        if constexpr (N == FXC) {
          asm volatile("s_waitcnt vmcnt(0)" : "+v"(fx) :: "memory");
          const unsigned f0 = fx[0], f1 = fx[1];
          const u64 bl = __builtin_amdgcn_ballot_w64(((f0 ^ tag0) | (f1 ^ tag1)) != 0);
          asm volatile("s_cmp_lg_u64 %1, 0\n\ts_cselect_b32 %0, 1, 0" : "=s"(late) : "s"(bl) : "scc");
        }
        if constexpr (N == FXP) { asm volatile("; LATE_BEGIN"); ldif(fx, ws, fxi, late ^ 1); asm volatile("; LATE_END"); }
        if constexpr (N > FXC && (N - FXC - 1) % ESP == 0 && (N - FXC - 1) / ESP < NE) early(std::integral_constant<int, (N - FXC - 1) / ESP>{}, late);
        if constexpr (N == 159) sfor<NE, 10>([&](auto ic) { early(ic, late); });
        if constexpr (N == 133) {
          SB();
          acc1[0] = mfma8(auga[0], augb, acc1[0]);
          acc1[1] = mfma8(auga[1], augb, acc1[1]);
        }
        SB();
      });
      asm volatile("; LATE_BEGIN\n\ts_cmp_eq_u32 %1, 0\n\ts_cbranch_scc1 .Lw%=\n\ts_waitcnt vmcnt(0)\n.Lw%=:\n\t; LATE_END" : "+v"(fx) : "s"(late) : "scc", "memory");
      TR(14);
      if constexpr (TRE == 0) TRD(15);
    }
  }
  TS(7);
  {
    PIN(lane);
    const int r = lane & 31, hi = lane >> 5;
    const unsigned l16 = lane * 16;
    const int rt = (xcd >> 1) * 16 + (idx & 15), cg = (xcd & 1) * 2 + (idx >> 4), kg = w & 3, hw = w >> 2;
    late = __builtin_amdgcn_readfirstlane(late);
    vw = l16 + fw * 1024; vh = l16 + fh * 1024 + (hq >> 1) * 8;
    if (late) {
      asm volatile("; LATE_BEGIN");
      const unsigned f0 = fx[0], f1 = fx[1];
      if (__builtin_amdgcn_ballot_w64(f0 != tag0 || f1 != tag1) != 0) wait_flags(ws, fxi, tag0, tag1);
      TS(5);
      sfor<0, 10>([&](auto ic) { early(ic, 0); });
      asm volatile("; LATE_END");
    }
    TS(8);
    auto ld_k = [&](KV& d, int t) {
      const char* p = ws + OFF_X + (t * 120 + 40) * 1024;
      ld16(d.kw, p, vw); ld8(d.kh, p, vh);
    };
    auto ld_v = [&](KV& d, int t) {
      const char* p = ws + OFF_X + (t * 120 + 80) * 1024;
      ld16(d.vw, p, vw); ld8(d.vh, p, vh);
    };
    SB();
    u4 ho[4];
    if (!dummy) {
      sfor<0, 2>([&](auto jc) {
        constexpr int j = decltype(jc)::value;
        gelu_tile(acc1[j], rstd, sig, ho[2 * j], ho[2 * j + 1]);
        st16<j * 2048>(hp, l16, ho[2 * j]);
        st16<j * 2048 + 1024>(hp, l16, ho[2 * j + 1]);
      });
    } else asm volatile("; LATE_BEGIN\n\ts_waitcnt vmcnt(0)\n\t; LATE_END" ::: "memory");
    TS(6);
    const int wv = 1536 + hi * 576 + r * 16, wh = 2688 + r * 16 + hi * 8;
    const int tv = (0x0A80084806000A88ull >> (16 * hq)) & 0xffff, tk = (0x0400020800000500ull >> (16 * hq)) & 0xffff;
    const int wc = (hi ? tv : tk) + (r << (3 + (hi | ((hq ^ (hq >> 1)) & 1))));
    const u2 cst = u2{hi ? 0x3f80u : 0x3f803f80u, hi ? 0u : 0x3f80u};
    auto wr_k = [&](const KV& d, char* p) { *(u4*)(p + l16) = d.kw; *(u2*)(p + 1024 + lane * 8) = d.kh; };
    auto wr_v = [&](const KV& d, char* p) {
      *(u4*)(p + wv) = d.vw; *(u2*)(p + wh) = d.vh;
      asm volatile("" ::: "memory");
      *(u2*)(p + wc) = cst;
    };
    int vrd;
    {
      const int pp = (lane >> 2 & 4) | (lane & 3);
      const int z = pp < 5 ? pp + (hq & 1) : 5 - 5 * (hq & 1);
      const int c = (z - (hq & 2)) & 7;
      vrd = 1536 + (c < 4 ? (c & 1) * 576 + (c >> 1) * 8 : 1152 + (z & 1) * 8) + (4 * hi + (lane >> 2 & 3)) * 16;
    }
    int fsel = (hi ^ (hq & 1)) * ((0x3213 >> (4 * hq)) & 3);
    PIN(fsel);
    auto set_q = [&](u2 v) {
      const unsigned a = v[0], c = v[1];
      qm = u4{fsel == 1 ? a : qm[0], fsel == 1 ? c : qm[1], fsel == 2 ? a : qm[2], fsel == 2 ? c : qm[3]};
      qa = u2{fsel == 3 ? a : qa[0], fsel == 3 ? c : qa[1]};
      PIN(qm); PIN(qa);
    };
#define TIE_KV(d) "+v"(d.kw), "+v"(d.kh), "+v"(d.vw), "+v"(d.vh)
    asm volatile("s_waitcnt vmcnt(%6)" : TIE_KV(sg0), "+v"(qm), "+v"(qa) : "n"(HSP ? 4 : 8) : "memory");
    wr_k(sg0, smem + L_E + w * KVT); wr_v(sg0, smem + L_E + w * KVT);
    set_q(u2{0u, 0u});
    WAIT_L0(); BARRIER();
    TS(9); TSC(26);
    if constexpr (PPE) __builtin_amdgcn_s_setprio(3);

    f16v sc, o = f16v{};
    {
      const u4 km = *(const u4*)(smem + L_E + l16);
      const u2 ka = *(const u2*)(smem + L_E + 1024 + lane * 8);
      f16v s0 = mfma16(km, qm, f16v{});
      s0 = mfma8(ka, qa, s0);
      float m = max16(s0);
      m = fmaxf(m, xor32(m));
      set_q(split3(m));
      sc = mfma16(km, qm, f16v{});
      sc = mfma8(ka, qa, sc);
    }
    u4 km = *(const u4*)(smem + L_E + KVT + l16);
    u2 ka = *(const u2*)(smem + L_E + KVT + 1024 + lane * 8);
    u4 v0 = rd_frag(smem + L_E + vrd, 128);
    u4 v1 = u4{0u, 0u, 0u, 0u}, v2;
    if constexpr (DB) v2 = rd_frag(smem + L_E + KVT + vrd, 128);
    const char* pkv = ws + OFF_X + ((16 + w) * 120 + 40) * 1024;
    u4 p0, p1 = u4{0u, 0u, 0u, 0u};
    float e12 = 0.f, e13 = 0.f, e14 = 0.f, e15 = 0.f;
    f16v acc[3] = {f16v{}, f16v{}, f16v{}};
    u4 ga[3][2], gb[3][5], wb[25];
    u2 ffw;
    const char* pa = ws + OFF_H + (rt * 108 + kg * 27) * 1024;
    const char* pb0 = ws + OFF_W2 + ((cg * 5 + 2 * hw) * 108 + kg * 27) * 1024;
    const char* pb4 = ws + OFF_W2 + ((cg * 5 + 4) * 108 + kg * 27) * 1024;
    const int h0 = hw * 1024, h1 = 1024 - h0;
    unsigned g0 = l16;
    auto g_ld = [&](auto mc) {
      constexpr int u = decltype(mc)::value / 5, i = decltype(mc)::value % 5, F = (u & 1) * 2048;
      const int o0 = u < 2 ? h0 : 4096;
      if constexpr (i == 0) ld16<F>(gb[u][0], pb0 + o0, g0);
      if constexpr (i == 1) ld16<F>(gb[u][1], pb0 + 108 * 1024 + o0, g0);
      if constexpr (i == 2) { ld16<F>(gb[u][2], pb4 + o0, g0); ld16<F>(ga[u][0], pa + o0, g0); }
      if constexpr (i == 3) ld16<F>(gb[u][3], pb0 + h1, g0);
      if constexpr (i == 4) { ld16<F>(gb[u][4], pb0 + 108 * 1024 + h1, g0); ld16<F>(ga[u][1], pa + h1, g0); }
      if constexpr (u == 2 && i == 2) { g0 += 5120; PIN(g0); }
    };
    auto g_mm = [&](int u, int i) { acc[i % 3] = mfma16(ga[u][i / 3], gb[u][i], acc[i % 3]); PIN(acc[i % 3]); };
#define TIE_G(u) "+v"(ga[u][0]), "+v"(ga[u][1]), "+v"(gb[u][0]), "+v"(gb[u][1]), "+v"(gb[u][2]), "+v"(gb[u][3]), "+v"(gb[u][4])
    const char* po0 = ws + OFF_WO + (cg * 5 + 2 * hw) * 40960;
    const char* po4 = ws + OFF_WO + (cg * 5 + 4) * 40960;
    auto ofr = [&](int i) {
      const int j = i >> 1, h = i & 1 ? h1 : h0;
      if (!ORT) return i < 8 ? kg * 1024 + (i & 6) * 4096 + h * 4 : 32768 + kg * 2048 + h;
      return j == 3 ? 32768 + kg * 2048 + h : kg * 1024 + (((b >> 1) + (j < 3 ? j + 1 : 0)) & 3) * 8192 + h * 4;
    };
    auto w_ld = [&](auto ic) {
      constexpr int i = decltype(ic)::value, k = i % 5;
      ld16<0>(wb[i], (k == 2 ? po4 : k % 3 == 1 ? po0 + 40960 : po0) + ofr(i / 5 * 2 + (k > 2)), l16);
    };
    u4 xa, ba;
    unsigned xc, bc, oa16, oa4;
    auto x_ld = [&]() {
      PIN(lane);
      const int oc = lane & 15, orow = (w >> 1) * 8 + (w & 1) * 2 + (lane >> 2 & 4) + (lane >> 5);
      oa16 = ((rt * 32 + orow) * DM + cg * 160 + oc * 8) * 2; oa4 = oa16 + 256 - oc * 12;
      ldp(xa, x, oa16); ldp(ba, b2 + cg * 160, oc * 16);
      ldp(xc, x, oa4); ldp(bc, b2 + cg * 160 + 128, oc * 4);
    };
    u2 ffx;
    const unsigned fai = OFF_FX + (b == 0 ? (w & 3) * 64 + lane : g * 16 + rb) * 64;
    const int fw1 = lane < 16 ? lane * 16 + (rt >> 2) : 128 + (cg * 5 >> 1) * 11 + lane;
    const unsigned fwi = OFF_FW + (lane < 49 ? fw1 : (head >> 1) * 16 + (qt >> 2)) * 64;

    auto stage = [&](auto kind, int s, int cur, int nxt) {
      constexpr int KD = decltype(kind)::value;
      const char* kc_ = smem + cur + l16; const char* kn_ = smem + nxt + l16;
      const char* ac_ = smem + cur + 1024 + lane * 8; const char* an_ = smem + nxt + 1024 + lane * 8;
      const char* vc_ = smem + cur + vrd; const char* vn_ = smem + nxt + vrd;
      auto hook = [&](auto pc) {
        constexpr int p = decltype(pc)::value, m = mslot(p), u = m / 5, i = m % 5;
        SB();
        if constexpr (p == 1) {
          if constexpr (KD == 0 || KD == 3) asm volatile("s_waitcnt vmcnt(%4)" : TIE_KV(sg) : "n"(KD ? 1 : HSP ? 2 : 6) : "memory");
          else if constexpr (KD == 2) asm volatile("s_waitcnt vmcnt(11)" : TIE_G(0) :: "memory");
          else asm volatile("s_waitcnt vmcnt(11)" : TIE_KV(sg), TIE_G(0) :: "memory");
        }
        if constexpr (KD != 2) {
          if constexpr (p == 2) wr_k(sg, smem + nxt + w * KVT);
          if constexpr (p == 3) wr_v(sg, smem + nxt + w * KVT);
          if constexpr (RPK && KD != 4) {
            if constexpr (p == 5) { ld16(sg.kw, pkv, vw); ld8(sg.kh, pkv, vh); pkv += 40960; asm volatile("" : "+s"(pkv)); }
            if constexpr (p == 6) { ld16(sg.vw, pkv, vw); ld8(sg.vh, pkv, vh); pkv += 920 * 1024; asm volatile("" : "+s"(pkv)); }
          } else {
            if constexpr (p == 5) { if constexpr (KD != 4) ld_k(sg, (s + 2) * 8 + w); else sfor<0, 2>(w_ld); }
            if constexpr (p == 6) { if constexpr (KD != 4) ld_v(sg, (s + 2) * 8 + w); else sfor<2, 4>(w_ld); }
          }
          if constexpr (p == 23 && KD != 0) { BARRIER(); if constexpr (PPE) __builtin_amdgcn_s_setprio(3); }
        }
        if constexpr (PPE && prio_at(KD, p) >= 0) __builtin_amdgcn_s_setprio(prio_at(KD, p));
        if constexpr (KD == 0) {
          if constexpr (p == 0) {
            char* cp = ws + OFF_O + (rb * 4 + kg) * 40960;
            st16<0>(cp + (g * 2 + hw) * 1024, l16, u4{~0u, ~0u, ~0u, ~0u});
            st16<0>(cp + 32768 + g * 512 + hw * 256, l16 & 255, u4{~0u, ~0u, ~0u, ~0u});
          }
          if constexpr (p == HFP) {
            WAIT_V(4); BARRIER();
            if constexpr (PPE) __builtin_amdgcn_s_setprio(3);
            st8(ws, OFF_FW + (g * 16 + rb) * 64, u2{tag0, tag1});
          }
        } else if constexpr (KD == 3) {
          if constexpr (p == FWP - 8) { ld8(ffw, ws, fwi); ld8(ffx, ws, fai); }
          if constexpr (p == FWP) {
            asm volatile("s_waitcnt vmcnt(0)" : "+v"(ffw), "+v"(ffx) :: "memory");
            const unsigned f0 = ffw[0], f1 = ffw[1], f2 = ffx[0], f3 = ffx[1];
            TS(3);
            if (__builtin_amdgcn_ballot_w64((((f0 ^ tag0) | (f1 ^ tag1)) | ((f2 ^ tag0) | (f3 ^ tag1))) != 0) != 0) {
              wait_flags(ws, fwi, tag0, tag1);
              wait_flags(ws, fai, tag0, tag1);
            }
            TS(4);
          }
          if constexpr (p > FWP) sfor<(p - FWP - 1) * 13 / FWN, (p - FWP > FWN ? FWN : p - FWP) * 13 / FWN>(g_ld);
        } else if constexpr (KD == 2) {
          if constexpr ((p & 1) && p < 10) g_mm(0, p >> 1);
          SB();
          if constexpr (p >= 1 && p <= 10) w_ld(std::integral_constant<int, 14 + p>{});
          if constexpr (p == XLP) x_ld();
        } else if constexpr (m >= 0) {
          if constexpr (m == 5) asm volatile("s_waitcnt vmcnt(15)" : TIE_G(1) :: "memory");
          if constexpr (m == 10) asm volatile("s_waitcnt vmcnt(18)" : "+v"(ga[2][0]), "+v"(gb[2][0]), "+v"(gb[2][1]), "+v"(gb[2][2]) :: "memory");
          g_mm(u, i);
          SB();
          if constexpr (m == 11) { gb[2][2] &= (unsigned)(hw - 1); PIN(gb[2][2]); }
          if constexpr (KD == 1 || m < 5) g_ld(std::integral_constant<int, m>{});
          else {
            constexpr int f = 7 * u - 3 + i + (i > 2);
            sfor<f, f + 1 + (i == 2 || i == 4)>(w_ld);
          }
        }
        SB();
      };
      sfor<0, 8>([&](auto tc) {
        constexpr int t = decltype(tc)::value;
        constexpr bool NX = KD != 2 || t < 7, NK = KD != 2 || t < 6;
        float e[16];
        f16v sn;
        auto gap = [&](auto qc, unsigned& d0, unsigned& d1, float a0, float a1, float a2, float a3) {
          constexpr int q = decltype(qc)::value;
          constexpr int hk = (KD == 1 || KD == 4 ? mslot(4 * t + q) >= 0 : KD == 2 && (q & 1) && 4 * t + q < 10) ? HK : 4;
          constexpr int cv = CV == 3 ? (q == 2 ? 2 : 0) : CV;
          if constexpr (cv >= 1) { d0 = pk_bf16(a0, a1); PIN(d0); }
          if constexpr (cv >= 2) { d1 = pk_bf16(a2, a3); PIN(d1); SB(); }
#pragma unroll
          for (int i = 0; i < hk; ++i) e[4 * q + i] = __builtin_amdgcn_exp2f(sc[4 * q + i]);
          hook(std::integral_constant<int, 4 * t + q>{});
#pragma unroll
          for (int i = hk; i < 4; ++i) e[4 * q + i] = __builtin_amdgcn_exp2f(sc[4 * q + i]);
          if constexpr (cv < 1) d0 = pk_bf16(a0, a1);
          if constexpr (cv < 2) d1 = pk_bf16(a2, a3);
          SB();
        };
        unsigned c0, c1;
        SB();
        if constexpr (W0 != 0) { __builtin_amdgcn_s_waitcnt(0xc07f | (W0 ? W0 - 1 : 0) << 8); SB(); }
        if constexpr (NX) { sn = mfma16(km, qm, f16v{}); PIN(sn); }
        SB();
        if constexpr (NK) km = *(const u4*)(t < 6 ? kc_ + (t + 2) * KVT : kn_ + (t - 6) * KVT);
        gap(std::integral_constant<int, 0>{}, c0, c1, e12, e13, e14, e15);
        p1[2] = c0; p1[3] = c1;
        if constexpr (NX) { sn = mfma8(ka, qa, sn); PIN(sn); }
        SB();
        if constexpr (NK) ka = *(const u2*)(t < 6 ? ac_ + (t + 2) * KVT : an_ + (t - 6) * KVT);
        gap(std::integral_constant<int, 1>{}, c0, c1, e[0], e[1], e[2], e[3]);
        p0[0] = c0; p0[1] = c1;
        o = mfma16(v1, p1, o);
        SB();
        v1 = rd_frag(vc_ + t * KVT + 256, 128);
        gap(std::integral_constant<int, 2>{}, c0, c1, e[4], e[5], e[6], e[7]);
        p0[2] = c0; p0[3] = c1;
        if constexpr (DB && (t & 1)) o = mfma16(v2, p0, o); else o = mfma16(v0, p0, o);
        SB();
        if constexpr (!DB && NX) v0 = rd_frag(t < 7 ? vc_ + (t + 1) * KVT : vn_, 128);
        if constexpr (DB && NK) {
          const u4 vv = rd_frag(t < 6 ? vc_ + (t + 2) * KVT : vn_ + (t - 6) * KVT, 128);
          if constexpr (t & 1) v2 = vv; else v0 = vv;
        }
        gap(std::integral_constant<int, 3>{}, c0, c1, e[8], e[9], e[10], e[11]);
        p1[0] = c0; p1[1] = c1;
        e12 = e[12]; e13 = e[13]; e14 = e[14]; e15 = e[15];
        if constexpr (NX) sc = sn;
      });
    };
    stage(std::integral_constant<int, 0>{}, 0, L_E, L_E + KVS);
    TS(10);
    stage(std::integral_constant<int, 3>{}, 1, L_E + KVS, L_E + 2 * KVS);
    {
      int cur = L_E + 2 * KVS;
#pragma unroll 1
      for (int s = 2; s < 6; ++s) {
        const int nxt = cur == L_E + 2 * KVS ? L_E : cur + KVS;
        stage(std::integral_constant<int, 1>{}, s, cur, nxt);
        cur = nxt;
      }
    }
    stage(std::integral_constant<int, 4>{}, 6, L_E, L_E + KVS);
    TS(11);
    stage(std::integral_constant<int, 2>{}, 7, L_E + KVS, 0);
    p1[2] = pk_bf16(e12, e13); p1[3] = pk_bf16(e14, e15);
    o = mfma16(v1, p1, o);
    TS(12); TSC(27);
    {
      float am = 0.f, chk = 0.f;
#pragma unroll
      for (int i = 0; i < 12; ++i) { am = fmaxf(am, fabsf(o[i])); chk += o[i] * 0.f; }
      const bool ok = (o[12] < 1e30f) && (am < 3e38f) && (chk == 0.f);
      if (__builtin_expect(__builtin_amdgcn_ballot_w64(!ok) != 0, 0)) {
        char* tp = smem + w * 4096;
        set_q(u2{0u, 0u});
        float m = -INFINITY;
        KV d;
        d.vw = u4{0u, 0u, 0u, 0u}; d.vh = u2{0u, 0u};
#pragma unroll 1
        for (int t = 0; t < 64; ++t) {
          ld_k(d, t);
          asm volatile("s_waitcnt vmcnt(0)" : TIE_KV(d) :: "memory");
          f16v s0 = mfma16(d.kw, qm, f16v{});
          s0 = mfma8(d.kh, qa, s0);
          m = fmaxf(m, max16(s0));
        }
        m = fmaxf(m, xor32(m));
        set_q(split3(m));
        o = f16v{};
#pragma unroll 1
        for (int t = 0; t < 64; ++t) {
          ld_k(d, t); ld_v(d, t);
          asm volatile("s_waitcnt vmcnt(0)" : TIE_KV(d) :: "memory");
          wr_k(d, tp); wr_v(d, tp);
          asm volatile("" ::: "memory");
          f16v s0 = mfma16(*(const u4*)(tp + l16), qm, f16v{});
          s0 = mfma8(*(const u2*)(tp + 1024 + lane * 8), qa, s0);
#pragma unroll
          for (int e = 0; e < 4; ++e) {
            p0[e] = pk_bf16(__builtin_amdgcn_exp2f(s0[2 * e]), __builtin_amdgcn_exp2f(s0[2 * e + 1]));
            p1[e] = pk_bf16(__builtin_amdgcn_exp2f(s0[8 + 2 * e]), __builtin_amdgcn_exp2f(s0[9 + 2 * e]));
          }
          o = mfma16(rd_frag(tp + vrd, 128), p0, o);
          o = mfma16(rd_frag(tp + vrd + 256, 128), p1, o);
          asm volatile("" ::: "memory");
        }
      }
    }
    {
      const float inv = 1.f / o[12];
      char* ob = ws + OFF_O + qt * 40960;
      auto pk = [&](int i) { return __builtin_elementwise_min(pk_bf16(o[i] * inv, o[i + 1] * inv), ~1u); };
      st16<0>(ob + head * 1024, l16, u4{pk(0), pk(2), pk(4), pk(6)});
      if (!hi) st8(ob + (32 + xcd) * 1024 + (hq >> 1) * 512 + (hq & 1) * 8, l16, u2{pk(8), pk(10)});
    }
    TS(13);
    if (B1 == 1 || (B1 == 2 && hw)) BARRIER();
    {
      u4 oa[10];
      const char* pq = ws + OFF_O + rt * 40960;
#pragma unroll
      for (int i = 0; i < 10; ++i) ld16<0>(oa[i], pq + ofr(i), l16);
      auto empty = [&](int i) {
        const unsigned a0 = oa[i][0], a1 = oa[i][1], a2 = oa[i][2], a3 = oa[i][3];
        return __builtin_amdgcn_ballot_w64(__builtin_elementwise_max(__builtin_elementwise_max(a0, a1), __builtin_elementwise_max(a2, a3)) == ~0u) != 0 ? 2u << i : 0u;
      };
      auto mm = [&](auto ic, unsigned skip) {
        constexpr int i = decltype(ic)::value, f = i / 2 * 5 + (i & 1) * 3;
        if constexpr (i & 1) mm2(acc[0], acc[1], oa[i], wb[f], wb[f + 1], skip);
        else mm3(acc[0], acc[1], acc[2], oa[i], wb[f], wb[f + 1], wb[f + 2], skip);
      };
      unsigned pend = 0;
      sfor<0, 10>([&](auto ic) {
        constexpr int i = decltype(ic)::value;
        wait_v<9 - i>(oa[i]);
        pend |= empty(i);
        mm(ic, pend);
      });
      PIN(xa); PIN(ba); PIN(xc); PIN(bc);
      TS(14);
      if (B1 == 2 && !hw) BARRIER();
#pragma unroll 1
      for (int spin = 0; pend != 0 && spin < (1 << 18); ++spin) {
        unsigned want = pend;
        if constexpr (PSL != 0) __builtin_amdgcn_s_sleep(PSL);
#pragma unroll
        for (int i = 0; i < 10; ++i)
          asm volatile("s_bitcmp1_b32 %3, %4\n\ts_cbranch_scc0 .Lld%=\n\tglobal_load_dwordx4 %0, %1, %2 sc1\n.Lld%=:"
                       : "+v"(oa[i]) : "v"(l16), "s"(pq + ofr(i)), "s"(want), "n"(i + 1) : "scc", "memory");
        asm volatile("s_waitcnt vmcnt(0)" : "+v"(oa[0]), "+v"(oa[1]), "+v"(oa[2]), "+v"(oa[3]), "+v"(oa[4]), "+v"(oa[5]),
                     "+v"(oa[6]), "+v"(oa[7]), "+v"(oa[8]), "+v"(oa[9]) :: "memory");
        unsigned now = 0;
#pragma unroll
        for (int i = 0; i < 10; ++i) now |= empty(i);
        const unsigned skip = ~((now & -now) - 1) | ((pend & -pend) - 1);
        sfor<0, 10>([&](auto ic) { mm(ic, skip & 2u << decltype(ic)::value); });
        pend = now;
      }
    }
    TS(15);
    asm volatile("s_nop 7\n\ts_nop 7\n\ts_mov_b32 m0, %0\n\ts_nop 0" :: "s"(lb + w * 4096), "v"(acc[0]), "v"(acc[1]), "v"(acc[2]) : "memory");
    sfor<0, 48>([&](auto ic) {
      constexpr int i = decltype(ic)::value, j = (i >> 4) == 0 ? 2 : (i >> 4) - 1;
      if constexpr (i == 32) asm volatile("s_mov_b32 m0, %0\n\ts_nop 0" :: "s"(lb + w * 4096 + 32768) : "memory");
      wr_tid<(i >= 16 ? 32768 : 0) + (i >= 32 ? 16 : 0) + (i & 15) * 256>(acc[j][i & 15]);
    });
    WAIT_L0(); BARRIER();
    if (threadIdx.x == 0 && b == 0) st8(ws, OFF_E, u2{tag0, tag1});
    {
      const int oc = lane & 15, oh = (lane >> 4) & 1;
      const int ri = ((w >> 1) * 4 + (w & 1) * 2 + hi) * 256 + oh * 128;
      const char* rp = smem + 32768 + ri + (oc >> 2 & 1) * 32784 + (oc >> 3) * 16384 + (oc & 3) * 32;
      f4 ra[4], rb[4];
      f2 rc[8];
#pragma unroll
      for (int i = 0; i < 4; ++i) {
        ra[i] = *(const f4*)(rp + i * 4096);
        rb[i] = *(const f4*)(rp + i * 4096 + 16);
      }
#pragma unroll
      for (int i = 0; i < 8; ++i) rc[i] = *(const f2*)(smem + ri + oc * 8 + i * 4096);
      auto fin = [&](float v0, float v1, unsigned xv, unsigned bv) {
        return pk_bf16(v0 + (bf_lo(xv) + bf_lo(bv)), v1 + (bf_hi(xv) + bf_hi(bv)));
      };
      const f4 va = (ra[0] + ra[1]) + (ra[2] + ra[3]), vb = (rb[0] + rb[1]) + (rb[2] + rb[3]);
      const f2 vc = ((rc[0] + rc[1]) + (rc[2] + rc[3])) + ((rc[4] + rc[5]) + (rc[6] + rc[7]));
      const u4 o16 = u4{fin(va[0], va[1], xa[0], ba[0]), fin(va[2], va[3], xa[1], ba[1]), fin(vb[0], vb[1], xa[2], ba[2]), fin(vb[2], vb[3], xa[3], ba[3])};
      const unsigned o4 = fin(vc[0], vc[1], xc, bc);
      st16<0>((char*)out, oa16, o16);
      asm volatile("global_store_dword %0, %1, %2 sc1" :: "v"(oa4), "v"(o4), "s"(out) : "memory");
    }
    TS(16); TSC(25); TS0B();
  }
}

extern "C" void kernel_launch(void* const* d_in, const int* in_sizes, int n_in, void* d_out, int out_size,
                              void* d_ws, size_t ws_size, hipStream_t stream) {
  mega_kernel<<<256, 512, 0, stream>>>((const u16*)d_in[0], (const u16*)d_in[1], (const u16*)d_in[2], (const u16*)d_in[4],
                                       (const u16*)d_in[5], (const u16*)d_in[6], (const u16*)d_in[7], (u16*)d_out, (char*)d_ws);
}
```
